```python
import torch

def _softmax(x, axis=-1):
    return torch.softmax(x, dim=axis)
def _gelu(x):
    return torch.nn.functional.gelu(x, approximate="tanh")
def _silu(x):
    return torch.nn.functional.silu(x)
def _sigmoid(x):
    return torch.sigmoid(x)
def _layernorm(x, axis=-1, eps=1e-5):
    m = x.mean(dim=axis, keepdim=True)
    v = x.var(dim=axis, keepdim=True, unbiased=False)
    return (x - m) / torch.sqrt(v + eps)
def _scatter_add(tgt, idx, src, axis=0):
    return tgt.clone().index_add_(axis, idx, src)
def _segment_sum(src, seg, n):
    out = torch.zeros((n,) + src.shape[1:], dtype=src.dtype)
    return out.index_add_(0, seg, src)
def _pad(x, pw):
    flat = tuple(v for pair in reversed(pw) for v in pair)
    return torch.nn.functional.pad(x, flat)

kernel_name = 'recurrent_scan__sliding_block_cached__dense_activation_gelu__decode'

def setup_inputs():

    _w = (lambda a: (lambda t: t.to(torch.bfloat16) if t.is_floating_point() else t)(torch.as_tensor(a)))
    def _A_setup():
        B, Sq, D = 8, 1, 1024
        Hq, Hkv, Sk, Dh = 32, 4, 4096, 128
        assert Hq % Hkv == 0, (Hq, Hkv)
        return {
            "x": _w((lambda *s: torch.randn(*s, dtype=torch.bfloat16) * 0.05)(B, Sq, D)),
            "k_cache": _w((lambda *s: torch.randn(*s, dtype=torch.bfloat16) * 3.2)(B, Hkv, Sk, Dh)),
            "v_cache": _w((lambda *s: torch.randn(*s, dtype=torch.bfloat16) * 3.2)(B, Hkv, Sk, Dh)),
            "wq": _w((lambda *s: torch.randn(*s, dtype=torch.bfloat16) * 3.2)(D, Hq * Dh) * (D ** -0.5)),
            "wo": _w((lambda *s: torch.randn(*s, dtype=torch.bfloat16) * 3.2)(Hq * Dh, D) * ((Hq * Dh) ** -0.5)),
        }
    _A_in = _A_setup()

    def _B_setup():
        B, S, D, F = 8, 1, 1024, 3584
        return {
            "x": _w((lambda *s: torch.randn(*s, dtype=torch.bfloat16) * 0.05)(B, S, D)),
            "w1": _w((lambda *s: torch.randn(*s, dtype=torch.bfloat16) * 3.2)(D, F) * (D ** -0.5)),
            "b1": _w((lambda *s: torch.randn(*s, dtype=torch.bfloat16) * 3.2)(F) * 0.01),
            "w2": _w((lambda *s: torch.randn(*s, dtype=torch.bfloat16) * 0.8)(F, D) * (F ** -0.5)),
            "b2": _w((lambda *s: torch.randn(*s, dtype=torch.bfloat16) * 0.8)(D) * 0.01),
        }
    _B_in = _B_setup()

    return {
        "A_x": _A_in["x"],
        "A_k_cache": _A_in["k_cache"],
        "A_v_cache": _A_in["v_cache"],
        "A_wq": _A_in["wq"],
        "A_wo": _A_in["wo"],
        "B_x": _B_in["x"],
        "B_w1": _B_in["w1"],
        "B_b1": _B_in["b1"],
        "B_w2": _B_in["w2"],
        "B_b2": _B_in["b2"],
    }

def reference(A_x, A_k_cache, A_v_cache, A_wq, A_wo, B_x, B_w1, B_b1, B_w2, B_b2):
    def _A_ref(x, k_cache, v_cache, wq, wo):
        B, Sq, D = x.shape
        Hq, Dh = 32, 128
        Hkv, Sk = k_cache.shape[1], k_cache.shape[2]
        W = min(4096, max(256, Sk // 4))
        rep = Hq // Hkv
        q = (lambda x, *d: x.permute(*d))((x @ wq).reshape(B, Sq, Hq, Dh), 0, 2, 1, 3)
        k = (lambda x, n, axis: torch.repeat_interleave(x, n, dim=axis))(k_cache[:, :, Sk - W:, :], rep, axis=1)
        v = (lambda x, n, axis: torch.repeat_interleave(x, n, dim=axis))(v_cache[:, :, Sk - W:, :], rep, axis=1)
        scores = torch.einsum("bhqd,bhkd->bhqk", q, k) * (Dh ** -0.5)
        p = _softmax(scores, axis=-1)
        o = torch.einsum("bhqk,bhkd->bhqd", p, v)
        return (lambda x, *d: x.permute(*d))(o, 0, 2, 1, 3).reshape(B, Sq, Hq * Dh) @ wo

    def _B_ref(x, w1, b1, w2, b2):
        h = torch.einsum("bsd,df->bsf", x, w1) + b1
        h = _gelu(h)
        return torch.einsum("bsf,fd->bsd", h, w2) + b2

    x = A_x
    xn = _layernorm(x, axis=-1)
    s = _A_ref(xn, A_k_cache, A_v_cache, A_wq, A_wo)
    g = _silu(_B_ref(xn, B_w1, B_b1, B_w2, B_b2))
    return x + s * g
```

```cpp
#include <hip/hip_runtime.h>
#include <hip/hip_bf16.h>

typedef unsigned short u16;
__device__ __forceinline__ float bf2f(u16 v) { return __uint_as_float(((unsigned)v) << 16); }

constexpr int B = 8, D = 1024, HQ = 32, HKV = 4, SK = 4096, DH = 128, W = 1024, F = 3584;

__global__ void k_ln(const u16* x, float* xn) {
    __shared__ float red[256];
    int b = blockIdx.x, t = threadIdx.x;
    float v[4]; float s = 0;
    for (int i = 0; i < 4; ++i) { v[i] = bf2f(x[b * D + t * 4 + i]); s += v[i]; }
    red[t] = s; __syncthreads();
    for (int o = 128; o > 0; o >>= 1) { if (t < o) red[t] += red[t + o]; __syncthreads(); }
    float m = red[0] / D; __syncthreads();
    float q = 0; for (int i = 0; i < 4; ++i) { float d = v[i] - m; q += d * d; }
    red[t] = q; __syncthreads();
    for (int o = 128; o > 0; o >>= 1) { if (t < o) red[t] += red[t + o]; __syncthreads(); }
    float var = red[0] / D;
    float r = 1.0f / sqrtf(var + 1e-5f);
    for (int i = 0; i < 4; ++i) xn[b * D + t * 4 + i] = (v[i] - m) * r;
}

template <int MODE>
__global__ void k_gemm(const float* in, const u16* w, const u16* bias, float* out, int K, int N) {
    __shared__ float red[4][8][64];
    int lane = threadIdx.x & 63, wv = threadIdx.x >> 6;
    int n = blockIdx.x * 64 + lane;
    float acc[8] = {0, 0, 0, 0, 0, 0, 0, 0};
    for (int k = wv; k < K; k += 4) {
        float wv_ = bf2f(w[(size_t)k * N + n]);
#pragma unroll
        for (int b = 0; b < 8; ++b) acc[b] += in[b * K + k] * wv_;
    }
    for (int b = 0; b < 8; ++b) red[wv][b][lane] = acc[b];
    __syncthreads();
    if (wv == 0) {
        for (int b = 0; b < 8; ++b) {
            float s = red[0][b][lane] + red[1][b][lane] + red[2][b][lane] + red[3][b][lane];
            if (MODE >= 1) s += bf2f(bias[n]);
            if (MODE == 1) { float u = 0.7978845608028654f * (s + 0.044715f * s * s * s); s = 0.5f * s * (1.0f + tanhf(u)); }
            out[b * N + n] = s;
        }
    }
}

__global__ void k_attn(const float* q, const u16* kc, const u16* vc, float* o) {
    __shared__ float p[W];
    __shared__ float red[256];
    __shared__ float qs[DH];
    int b = blockIdx.x / HQ, h = blockIdx.x % HQ, hk = h / (HQ / HKV), t = threadIdx.x;
    if (t < DH) qs[t] = q[b * HQ * DH + h * DH + t];
    __syncthreads();
    const u16* kb = kc + ((size_t)(b * HKV + hk) * SK + (SK - W)) * DH;
    const u16* vb = vc + ((size_t)(b * HKV + hk) * SK + (SK - W)) * DH;
    float mx = -INFINITY;
    for (int j = t; j < W; j += 256) {
        float s = 0;
        for (int d = 0; d < DH; ++d) s += qs[d] * bf2f(kb[(size_t)j * DH + d]);
        s *= 0.08838834764831845f;
        p[j] = s; mx = fmaxf(mx, s);
    }
    red[t] = mx; __syncthreads();
    for (int o2 = 128; o2 > 0; o2 >>= 1) { if (t < o2) red[t] = fmaxf(red[t], red[t + o2]); __syncthreads(); }
    mx = red[0]; __syncthreads();
    float sum = 0;
    for (int j = t; j < W; j += 256) { float e = expf(p[j] - mx); p[j] = e; sum += e; }
    red[t] = sum; __syncthreads();
    for (int o2 = 128; o2 > 0; o2 >>= 1) { if (t < o2) red[t] += red[t + o2]; __syncthreads(); }
    sum = red[0]; __syncthreads();
    int d = t & 127, half = t >> 7;
    float acc = 0;
    for (int j = half; j < W; j += 2) acc += p[j] * bf2f(vb[(size_t)j * DH + d]);
    red[t] = acc; __syncthreads();
    if (t < 128) o[b * HQ * DH + h * DH + t] = (red[t] + red[t + 128]) / sum;
}

__global__ void k_final(const u16* x, const float* s, const float* y, __hip_bfloat16* out) {
    int i = blockIdx.x * 256 + threadIdx.x;
    float g = y[i]; g = g / (1.0f + expf(-g));
    out[i] = __float2bfloat16(bf2f(x[i]) + s[i] * g);
}

extern "C" void kernel_launch(void* const* d_in, const int* in_sizes, int n_in,
                              void* d_out, int out_size, void* d_ws, size_t ws_size,
                              hipStream_t stream) {
    const u16* x = (const u16*)d_in[0];
    const u16* kc = (const u16*)d_in[1];
    const u16* vc = (const u16*)d_in[2];
    const u16* wq = (const u16*)d_in[3];
    const u16* wo = (const u16*)d_in[4];
    const u16* w1 = (const u16*)d_in[6];
    const u16* b1 = (const u16*)d_in[7];
    const u16* w2 = (const u16*)d_in[8];
    const u16* b2 = (const u16*)d_in[9];
    float* ws = (float*)d_ws;
    float* xn = ws; float* q = xn + 8 * D; float* h = q + 8 * 4096; float* o = h + 8 * F; float* s = o + 8 * 4096; float* y = s + 8 * D;
    k_ln<<<8, 256, 0, stream>>>(x, xn);
    k_gemm<0><<<4096 / 64, 256, 0, stream>>>(xn, wq, nullptr, q, D, 4096);
    k_gemm<1><<<F / 64, 256, 0, stream>>>(xn, w1, b1, h, D, F);
    k_attn<<<B * HQ, 256, 0, stream>>>(q, kc, vc, o);
    k_gemm<0><<<D / 64, 256, 0, stream>>>(o, wo, nullptr, s, 4096, D);
    k_gemm<2><<<D / 64, 256, 0, stream>>>(h, w2, b2, y, F, D);
    k_final<<<8 * D / 256, 256, 0, stream>>>(x, s, y, (__hip_bfloat16*)d_out);
}
```

```cpp
#include <hip/hip_runtime.h>
#include <hip/hip_bf16.h>
#include <stdint.h>

#define GAS __attribute__((address_space(1)))
#define LAS __attribute__((address_space(3)))
typedef unsigned short u16;
typedef unsigned v4u __attribute__((ext_vector_type(4)));
typedef unsigned v2u __attribute__((ext_vector_type(2)));
typedef float f32x4 __attribute__((ext_vector_type(4)));
typedef float f32x2 __attribute__((ext_vector_type(2)));
typedef __bf16 bf16x8 __attribute__((ext_vector_type(8)));
typedef __bf16 bf16x2 __attribute__((ext_vector_type(2)));

#define MK_STAMPS 0
#define MK_WCH 6
#define MK_ORDER 0
#define MK_CSLEEP 0
#define MK_DQ 16
#define MK_DO 16
#define MK_DH 16
#define MK_DF 8

namespace mk {

constexpr int B = 8, D = 1024, SK = 4096, DH = 128, WIN = 1024, F = 3584, NQ = 4096;
constexpr int NCH = 8, CH = 4, WCH = MK_WCH;

constexpr int WS_EPOCH = 0;
constexpr int QP_ROW = 16384, QP_OFF = 4096;
constexpr int HP_ROW = 14336, HP_OFF = QP_OFF + 32 * QP_ROW;
constexpr int OP_OFF = HP_OFF + 32 * HP_ROW;
constexpr int OM_OFF = OP_OFF + 32 * 8 * 8 * 512;
constexpr int SP_OFF = OM_OFF + 32 * 8 * 8 * 16;
constexpr int YP_OFF = SP_OFF + 4 * 64 * 8192;
constexpr int WS_END = YP_OFF + 4 * 56 * 8192;
constexpr int ST_OFF = WS_END;
constexpr int WS_BYTES = ST_OFF + 256 * 8 * 16 * 8;

constexpr int XN_STR = 2048 + 16, XN_OFF = 0;
constexpr int QS_STR = 256 + 16, QS_OFF = XN_OFF + 8 * XN_STR;
constexpr int AC_STR = 128 + 16, OA_OFF = QS_OFF + 8 * QS_STR;
constexpr int HA_OFF = OA_OFF + 8 * AC_STR;
constexpr int ZR_OFF = HA_OFF + 8 * AC_STR;
constexpr int KV_OFF = ZR_OFF + 128;
constexpr int ST1_OFF = KV_OFF + 4096;
constexpr int FL_OFF = ST1_OFF + 512;
constexpr int KS_OFF = FL_OFF + 256;
constexpr int LDS_USED = KS_OFF + 32768;
constexpr int LDS_BYTES = 96 * 1024;
static_assert(LDS_USED + 1024 <= LDS_BYTES && (KS_OFF % 16) == 0 && (KV_OFF % 16) == 0 && (ZR_OFF % 16) == 0, "lds");
enum { FL_PROG = 0  , FL_XN = 8, FL_Q = 9, FL_KV1 = 10, FL_O = 11, FL_H = 12, FL_T = 13, FL_TV = 14, FL_HT = 15, FL_OT = 16, FL_ST = 17, FL_QT = 18, FL_YT = 19 };

struct Params {
    const u16 *x, *kc, *vc, *wq, *wo, *w1, *b1, *w2, *b2;
    u16* out;
    unsigned char* ws;
};

constexpr unsigned kSpinCap = 1u << 16;
constexpr int AUX_NT = 2, AUX_SC1 = 16;

__device__ __forceinline__ float bf_lo(unsigned u) { return __builtin_bit_cast(float, u << 16); }
__device__ __forceinline__ float bf_hi(unsigned u) { return __builtin_bit_cast(float, u & 0xffff0000u); }
__device__ __forceinline__ unsigned pk_bf16(float lo, float hi) {
    f32x2 v = {lo, hi};
    return __builtin_bit_cast(unsigned, __builtin_convertvector(v, bf16x2));
}
__device__ __forceinline__ f32x4 mfma(v4u a, v4u b, f32x4 c) {
    return __builtin_amdgcn_mfma_f32_16x16x32_bf16(__builtin_bit_cast(bf16x8, a), __builtin_bit_cast(bf16x8, b), c, 0, 0, 0);
}
__device__ __forceinline__ unsigned f2u(float f) { return __builtin_bit_cast(unsigned, f); }
__device__ __forceinline__ float u2f(unsigned u) { return __builtin_bit_cast(float, u); }

#define CBAR() asm volatile("" ::: "memory")
template <int N> __device__ __forceinline__ void waitvm() { asm volatile("s_waitcnt vmcnt(%0)" ::"i"(N) : "memory"); }

__device__ __forceinline__ void lds_set(LAS unsigned* p, unsigned v) {
    CBAR();
    __hip_atomic_store(p, v, __ATOMIC_RELAXED, __HIP_MEMORY_SCOPE_WORKGROUP);
    CBAR();
}
__device__ __forceinline__ void lds_inc(LAS unsigned* p, int lane) {
    CBAR();
    if (lane == 0) __hip_atomic_fetch_add(p, 1u, __ATOMIC_RELAXED, __HIP_MEMORY_SCOPE_WORKGROUP);
    CBAR();
}
__device__ __forceinline__ unsigned lds_get(LAS unsigned* p) {
    return (unsigned)__builtin_amdgcn_readfirstlane(__hip_atomic_load(p, __ATOMIC_RELAXED, __HIP_MEMORY_SCOPE_WORKGROUP));
}
__device__ __forceinline__ void lds_wait_ge(LAS unsigned* p, unsigned v) {
#pragma nounroll
    for (unsigned spins = 0; spins < kSpinCap; ++spins) {
        if (lds_get(p) >= v) break;
        __builtin_amdgcn_s_sleep(1);
    }
    CBAR();
}

__device__ __forceinline__ float xrow16_max(float x) {
    auto s = __builtin_amdgcn_permlane16_swap(f2u(x), f2u(x), false, false);
    x = fmaxf(u2f(s[0]), u2f(s[1]));
    auto t = __builtin_amdgcn_permlane32_swap(f2u(x), f2u(x), false, false);
    return fmaxf(u2f(t[0]), u2f(t[1]));
}
__device__ __forceinline__ float xrow16_sum(float x) {
    auto s = __builtin_amdgcn_permlane16_swap(f2u(x), f2u(x), false, false);
    x = u2f(s[0]) + u2f(s[1]);
    auto t = __builtin_amdgcn_permlane32_swap(f2u(x), f2u(x), false, false);
    return u2f(t[0]) + u2f(t[1]);
}
template <int CTRL> __device__ __forceinline__ float dpp(float x) {
    return u2f(__builtin_amdgcn_mov_dpp(f2u(x), CTRL, 0xf, 0xf, true));
}
constexpr int XOR1 = 0xB1, XOR2 = 0x4E, XOR7 = 0x141, XOR8 = 0x128;
__device__ __forceinline__ float row16_sum(float x) {
    x += dpp<XOR1>(x); x += dpp<XOR2>(x); x += dpp<XOR7>(x); x += dpp<XOR8>(x);
    return x;
}
__device__ __forceinline__ float wave_sum(float x) { return xrow16_sum(row16_sum(x)); }

#define STAMP(i) do {} while (0)

__device__ __forceinline__ void transpose8(v4u* r) {
    v4u o[8];
#pragma unroll
    for (int t = 0; t < 4; ++t)
#pragma unroll
        for (int jp = 0; jp < 4; ++jp) {
            const unsigned x0 = r[2 * jp][t], x1 = r[2 * jp + 1][t];
            o[2 * t][jp] = __builtin_amdgcn_perm(x1, x0, 0x05040100u);
            o[2 * t + 1][jp] = __builtin_amdgcn_perm(x1, x0, 0x07060302u);
        }
#pragma unroll
    for (int e = 0; e < 8; ++e) { asm volatile("" : "+v"(o[e])); r[e] = o[e]; }
}

__device__ __forceinline__ void lds_wait_ge_fast(LAS unsigned* p, unsigned v) {
#pragma nounroll
    for (unsigned spins = 0; spins < 16u * kSpinCap; ++spins) {
        if (lds_get(p) >= v) break;
    }
    CBAR();
}
template <int K, class LD>
__device__ __forceinline__ void stream_head(LD& ld, v4u (&r)[32], LAS unsigned* prev) {
    if constexpr (K == 0) lds_wait_ge(prev, NCH + K - WCH > 0 ? NCH + K - WCH : 1);
    lds_wait_ge_fast(prev, NCH + K - WCH + 1);
#pragma unroll
    for (int i = 0; i < CH; ++i) r[K * CH + i] = ld(K * CH + i);
    __builtin_amdgcn_sched_barrier(0);
    if constexpr (K + 1 < WCH) stream_head<K + 1>(ld, r, prev);
}
template <int K, class LD, class ON>
__device__ __forceinline__ void stream_step(LD& ld, ON& on, v4u (&r)[32], LAS unsigned* me) {
    if constexpr (K < NCH) {
        waitvm<(WCH - 1) * CH>();
        lds_set(me, K - WCH + 1);
#pragma unroll
        for (int i = 0; i < CH; ++i) r[K * CH + i] = ld(K * CH + i);
        __builtin_amdgcn_sched_barrier(0);
        on.template run<K - WCH>(r);
        __builtin_amdgcn_sched_barrier(0);
    } else {
        waitvm<(NCH + WCH - 1 - K) * CH>();
        lds_set(me, K - WCH + 1);
        on.template run<K - WCH>(r);
        __builtin_amdgcn_sched_barrier(0);
    }
    if constexpr (K + 1 < NCH + WCH) stream_step<K + 1>(ld, on, r, me);
}

template <int N>
__device__ __forceinline__ void gather(__amdgpu_buffer_rsrc_t rs, const int (&voff)[N], unsigned tag, v4u (&g)[N]) {
    unsigned pend = (1u << N) - 1u;
#pragma nounroll
    for (unsigned spins = 0; spins < kSpinCap; ++spins) {
        CBAR();
#pragma unroll
        for (int i = 0; i < N; ++i)
            if (pend & (1u << i)) g[i] = __builtin_amdgcn_raw_buffer_load_b128(rs, voff[i], 0, AUX_SC1);
#pragma unroll
        for (int i = 0; i < N; ++i)
            if ((pend & (1u << i)) && __all(g[i].y == tag && g[i].w == tag)) pend &= ~(1u << i);
        if (pend == 0u) break;
    }
    CBAR();
}

struct GemmOn {
    LAS unsigned* flag; unsigned need;
    const LAS unsigned char* pa0; const LAS unsigned char* pa1; int step;
    f32x4 (&acc)[8];
    v4u (&a)[4];
    int done;
    __device__ __forceinline__ void load_a() {
        a[0] = *(const LAS v4u*)(pa0); a[1] = *(const LAS v4u*)(pa1);
        a[2] = *(const LAS v4u*)(pa0 + step); a[3] = *(const LAS v4u*)(pa1 + step);
    }
    template <int G> __device__ __forceinline__ void mul(v4u (&r)[32]) {
#pragma unroll
        for (int e = 0; e < 8; ++e) acc[e] = mfma(a[G], r[8 * G + e], acc[e]);
    }
    template <int C> __device__ __forceinline__ void run(v4u (&r)[32]) {
        if constexpr (C & 1) {
            constexpr int G = C >> 1;
            transpose8(&r[8 * G]);
            if (done < 0 && lds_get(flag) >= need) {
                CBAR();
                load_a();
                done = 0;
                if constexpr (G >= 1) mul<0>(r);
                if constexpr (G >= 2) mul<1>(r);
                if constexpr (G >= 3) mul<2>(r);
            }
            if (done >= 0) mul<G>(r);
        }
    }
    __device__ __forceinline__ void finish(v4u (&r)[32]) {
        if (done < 0) {
            lds_wait_ge(flag, need);
            load_a();
            mul<0>(r); mul<1>(r); mul<2>(r); mul<3>(r);
        }
    }
};
struct KvOn {
    LAS unsigned char* ks; int lane;
    template <int C> __device__ __forceinline__ void run(v4u (&r)[32]) {
        if constexpr (C == 1 || C == 3) {
            constexpr int H = C >> 1;
            const int g = lane >> 4, c = lane & 15;
#pragma unroll
            for (int i = 0; i < 8; ++i) {
                const int key = 32 * H + 4 * i + g;
                *(LAS v4u*)(ks + key * 256 + ((c ^ ((4 * i + g) & 15)) * 16)) = r[8 * H + i];
            }
            CBAR();
#pragma unroll
            for (int t = 0; t < 2; ++t)
#pragma unroll
                for (int s = 0; s < 4; ++s) r[8 * H + 4 * t + s] = *(const LAS v4u*)(ks + (32 * H + 16 * t + c) * 256 + (((4 * s + g) ^ c) * 16));
        }
        if constexpr (C == 5) transpose8(&r[16]);
        if constexpr (C == 7) transpose8(&r[24]);
    }
};

__device__ __forceinline__ void fold_halves(const f32x4 (&acc)[8], int lane, float (&res)[8]) {
    const bool lo = (lane & 8) == 0;
#pragma unroll
    for (int e = 0; e < 8; ++e) {
        auto s = __builtin_amdgcn_permlane32_swap(f2u(acc[e][0]), f2u(acc[e][1]), false, false);
        auto t = __builtin_amdgcn_permlane32_swap(f2u(acc[e][2]), f2u(acc[e][3]), false, false);
        float s1 = u2f(s[0]) + dpp<XOR8>(u2f(s[1]));
        float s2 = u2f(t[1]) + dpp<XOR8>(u2f(t[0]));
        asm volatile("" : "+v"(s1), "+v"(s2));
        res[e] = lo ? s1 : s2;
    }
}

__device__ __forceinline__ void ln_rows(const v4u (&xv)[4], LAS unsigned char* dst, int lane) {
    float v[2][16], mean[2], rstd[2];
#pragma unroll
    for (int k = 0; k < 2; ++k) {
#pragma unroll
        for (int q = 0; q < 4; ++q) {
            v[k][2 * q] = bf_lo(xv[2 * k][q]); v[k][2 * q + 1] = bf_hi(xv[2 * k][q]);
            v[k][8 + 2 * q] = bf_lo(xv[2 * k + 1][q]); v[k][8 + 2 * q + 1] = bf_hi(xv[2 * k + 1][q]);
        }
        float s4[4];
#pragma unroll
        for (int q = 0; q < 4; ++q) s4[q] = (v[k][4 * q] + v[k][4 * q + 1]) + (v[k][4 * q + 2] + v[k][4 * q + 3]);
        mean[k] = (s4[0] + s4[1]) + (s4[2] + s4[3]);
    }
    mean[0] = wave_sum(mean[0]) * (1.0f / D);
    mean[1] = wave_sum(mean[1]) * (1.0f / D);
#pragma unroll
    for (int k = 0; k < 2; ++k) {
#pragma unroll
        for (int i = 0; i < 16; ++i) v[k][i] -= mean[k];
        float s4[4];
#pragma unroll
        for (int q = 0; q < 4; ++q)
            s4[q] = fmaf(v[k][4 * q], v[k][4 * q], v[k][4 * q + 1] * v[k][4 * q + 1]) + fmaf(v[k][4 * q + 2], v[k][4 * q + 2], v[k][4 * q + 3] * v[k][4 * q + 3]);
        rstd[k] = (s4[0] + s4[1]) + (s4[2] + s4[3]);
    }
    rstd[0] = wave_sum(rstd[0]);
    rstd[1] = wave_sum(rstd[1]);
#pragma unroll
    for (int k = 0; k < 2; ++k) {
        const float rs_ = 1.0f / sqrtf(rstd[k] * (1.0f / D) + 1e-5f);
#pragma unroll
        for (int h = 0; h < 2; ++h) {
            const v4u o = {pk_bf16(v[k][h * 8] * rs_, v[k][h * 8 + 1] * rs_), pk_bf16(v[k][h * 8 + 2] * rs_, v[k][h * 8 + 3] * rs_),
                           pk_bf16(v[k][h * 8 + 4] * rs_, v[k][h * 8 + 5] * rs_), pk_bf16(v[k][h * 8 + 6] * rs_, v[k][h * 8 + 7] * rs_)};
            *(LAS v4u*)(dst + k * XN_STR + h * 1024 + lane * 16) = o;
        }
    }
}
__device__ __forceinline__ void load_x_rows(const u16* x, int row0, int lane, v4u (&xv)[4]) {
    const GAS unsigned char* xp = (const GAS unsigned char*)x + row0 * 2048 + lane * 16;
#pragma unroll
    for (int i = 0; i < 4; ++i) xv[i] = *(const GAS v4u*)(xp + i * 1024);
}

__device__ __forceinline__ float gelu_tanh(float s) {
    const float u = 0.7978845608028654f * (s + 0.044715f * s * s * s);
    const float e = __expf(2.0f * u);
    const float th = 1.0f - 2.0f / (e + 1.0f);
    return 0.5f * s * (1.0f + th);
}
__device__ __forceinline__ float silu(float y) { return y / (1.0f + __expf(-y)); }

__global__ void __launch_bounds__(512, 2) fused_kernel(Params p) {
    __shared__ __attribute__((aligned(16))) unsigned char smem[LDS_BYTES];
    LAS unsigned char* L = (LAS unsigned char*)smem;
    LAS unsigned* FL = (LAS unsigned*)(L + FL_OFF);
    const int tid = threadIdx.x, lane = tid & 63, w = __builtin_amdgcn_readfirstlane(tid >> 6), bid = blockIdx.x;
    const int g = lane >> 4, c = lane & 15;
    const __amdgpu_buffer_rsrc_t rs = __builtin_amdgcn_make_buffer_rsrc((void*)p.ws, 0, WS_BYTES, 0x00020000);
    STAMP(0);
    constexpr float C2 = 0.08838834764831845f * 1.4426950408889634f;

    const int xcd = bid & 7, idx = bid >> 3;
    const bool has_w1 = bid < 224;
    const bool has_w2 = idx < 28;
    const int ks_o = xcd * 8 + (idx >> 2), nsl_o = idx & 3;
    const int t2 = xcd * 28 + idx, ks_2 = t2 >> 2, nsl_2 = t2 & 3;
    int prev;
    if (MK_ORDER == 0) prev = (w == 4 && !has_w1) ? 2 : w - 1;
    else prev = w == 3 ? 0 : w == 1 ? (has_w1 ? 3 : 0) : w == 5 ? 2 : w == 4 ? (has_w2 ? 5 : 2) : w - 1;

    const bool is_gemm = (w == 0 || w == 3 || w == 4 || w == 5);
    if (is_gemm) {
        v4u r[32];
        const bool ta = (w == 0 || w == 3);
        const u16* W = w == 0 ? p.wq : w == 3 ? p.w1 : w == 4 ? p.wo : p.w2;
        const int ldw = w == 0 ? NQ * 2 : w == 3 ? F * 2 : D * 2;
        const int nbytes = w == 3 || w == 5 ? D * F * 2 : D * NQ * 2;
        const int ks = ta ? (bid & 3) : w == 4 ? ks_o : ks_2;
        const int ns = ta ? (bid >> 2) : w == 4 ? nsl_o : nsl_2;
        const __amdgpu_buffer_rsrc_t rw = __builtin_amdgcn_make_buffer_rsrc((void*)W, 0, nbytes, 0x00020000);
        const int voffw = ta ? (ks * 256 + 32 * (c >> 3) + 8 * g) * ldw + (ns * 64 + 8 * (c & 7)) * 2
                             : (ks * 64 + 8 * g) * ldw + (ns * 256 + 8 * c) * 2;
        const int go1 = ta ? 64 * ldw : 256, go2 = ta ? 128 * ldw : 32 * ldw;
        auto ldg = [&](int i) {
            return __builtin_amdgcn_raw_buffer_load_b128(rw, voffw, (i & 7) * ldw + ((i >> 3) & 1) * go1 + (i >> 4) * go2, AUX_NT);
        };
        if (w == 0) {
            FL[lane] = 0u;
            if (lane < 32) ((LAS unsigned*)(L + ZR_OFF))[lane] = 0u;
#pragma unroll
            for (int i = 0; i < WCH * CH; ++i) r[i] = ldg(i);
            __builtin_amdgcn_sched_barrier(0);
            __syncthreads();
        } else {
            v4u xv[4];
            if (w >= 4) load_x_rows(p.x, (w - 4) * 2, lane, xv);
            __syncthreads();
            if (w >= 4) { ln_rows(xv, L + XN_OFF + (w - 4) * 2 * XN_STR, lane); lds_inc(FL + FL_XN, lane); STAMP(8); }
        }
        const bool has = w == 3 ? has_w1 : w == 5 ? has_w2 : true;
        LAS unsigned* trig = FL + (w == 0 ? FL_QT : w == 3 ? FL_HT : w == 4 ? FL_ST : FL_YT);
        if (has) {
            unsigned bbias = 0;
            if (w != 0) stream_head<0>(ldg, r, FL + FL_PROG + prev);
            f32x4 acc[8];
#pragma unroll
            for (int e = 0; e < 8; ++e) acc[e] = f32x4{0.f, 0.f, 0.f, 0.f};
            v4u a[4];
            const LAS unsigned char* pa0; const LAS unsigned char* pa1; int step;
            if (ta) {
                pa0 = L + XN_OFF + ks * 512 + (c & 7) * XN_STR + (32 * (c >> 3) + 8 * g) * 2; pa1 = pa0 + 128; step = 256;
            } else {
                const LAS unsigned char* act = L + (w == 4 ? OA_OFF : HA_OFF) + (c & 7) * AC_STR + g * 16;
                const LAS unsigned char* zr = L + ZR_OFF + g * 16;
                pa0 = c < 8 ? act : zr; pa1 = c < 8 ? zr : act; step = 64;
            }
            GemmOn on{FL + (ta ? FL_XN : w == 4 ? FL_O : FL_H), ta ? 4u : w == 4 ? 2u : 1u, pa0, pa1, step, acc, a, -1};
            stream_step<WCH>(ldg, on, r, FL + FL_PROG + w);
            STAMP(2);
            on.finish(r);
            STAMP(3);
            lds_wait_ge(FL + FL_T, 1);
            const unsigned tag = __builtin_amdgcn_readfirstlane(FL[FL_TV]);
            __builtin_amdgcn_s_setprio(3);
            if (ta) {
                float res[8];
                fold_halves(acc, lane, res);
                const int b = 4 * (g & 1) + (g >> 1) + 2 * (c >> 3);
                const int o = (w == 0 ? QP_OFF + (ks * 8 + b) * QP_ROW : HP_OFF + (ks * 8 + b) * HP_ROW) + ns * 256 + (c & 7) * 16;
#pragma unroll
                for (int u = 0; u < 2; ++u)
                    __builtin_amdgcn_raw_buffer_store_b128(v4u{pk_bf16(res[4 * u], res[4 * u + 1]), tag, pk_bf16(res[4 * u + 2], res[4 * u + 3]), tag}, rs, o, u * 128, AUX_SC1);
            } else {
                const int o = (w == 4 ? SP_OFF + (ns * 64 + ks) * 8192 : YP_OFF + (ns * 56 + ks) * 8192) + lane * 16;
#pragma unroll
                for (int rr = 0; rr < 4; ++rr)
#pragma unroll
                    for (int eh = 0; eh < 2; ++eh)
                        __builtin_amdgcn_raw_buffer_store_b128(
                            v4u{pk_bf16(acc[4 * eh][rr], acc[4 * eh + 1][rr]), tag, pk_bf16(acc[4 * eh + 2][rr], acc[4 * eh + 3][rr]), tag}, rs, o,
                            (2 * rr + eh) * 1024, AUX_SC1);
            }
            lds_set(trig, 1);
            STAMP(4);
            (void)bbias;
        } else {
            if (w == 3) lds_wait_ge(FL + FL_PROG + (MK_ORDER == 0 ? 4 : 1), NCH / 2);
            else lds_wait_ge(FL + FL_PROG + 4, NCH);
            lds_set(trig, 1);
        }
        if (w == 3 && has_w2) {
            lds_wait_ge(FL + FL_T, 1);
            const unsigned tag = __builtin_amdgcn_readfirstlane(FL[FL_TV]);
            const int n4 = 8 * (c & 7) + 4 * (c >> 3);
            const v2u bb = *(const GAS v2u*)(p.b1 + ks_2 * 64 + n4);
            int vo[8];
#pragma unroll
            for (int i = 0; i < 8; ++i) vo[i] = HP_OFF + ((i >> 1) * 8 + (i & 1) * 4 + g) * HP_ROW + ks_2 * 256 + c * 16;
            __builtin_amdgcn_s_sleep(MK_DH);
            v4u h8[8];
            gather<8>(rs, vo, tag, h8);
            STAMP(5);
#pragma unroll
            for (int bh = 0; bh < 2; ++bh) {
                float v0 = (bf_lo(h8[bh].x) + bf_lo(h8[2 + bh].x)) + (bf_lo(h8[4 + bh].x) + bf_lo(h8[6 + bh].x)) + bf_lo(bb.x);
                float v1 = (bf_hi(h8[bh].x) + bf_hi(h8[2 + bh].x)) + (bf_hi(h8[4 + bh].x) + bf_hi(h8[6 + bh].x)) + bf_hi(bb.x);
                float v2 = (bf_lo(h8[bh].z) + bf_lo(h8[2 + bh].z)) + (bf_lo(h8[4 + bh].z) + bf_lo(h8[6 + bh].z)) + bf_lo(bb.y);
                float v3 = (bf_hi(h8[bh].z) + bf_hi(h8[2 + bh].z)) + (bf_hi(h8[4 + bh].z) + bf_hi(h8[6 + bh].z)) + bf_hi(bb.y);
                *(LAS v2u*)(L + HA_OFF + (bh * 4 + g) * AC_STR + n4 * 2) = v2u{pk_bf16(gelu_tanh(v0), gelu_tanh(v1)), pk_bf16(gelu_tanh(v2), gelu_tanh(v3))};
            }
            lds_set(FL + FL_H, 1);
            STAMP(6);
        }
    } else if (w == 1 || w == 2) {
        const int gid = xcd * 4 + (idx >> 3), sp = idx & 7;
        const size_t kvbase = ((size_t)gid * SK + (SK - WIN) + sp * 128 + (w - 1) * 64) * (DH * 2);
        const __amdgpu_buffer_rsrc_t rk = __builtin_amdgcn_make_buffer_rsrc((void*)((const unsigned char*)p.kc + kvbase), 0, 64 * 256, 0x00020000);
        const __amdgpu_buffer_rsrc_t rv = __builtin_amdgcn_make_buffer_rsrc((void*)((const unsigned char*)p.vc + kvbase), 0, 64 * 256, 0x00020000);
        const int vvo = g * 1024 + c * 16;
        auto ld = [&](int i) {
            if (i < 16) return __builtin_amdgcn_raw_buffer_load_b128(rk, lane * 16, i * 1024, AUX_NT);
            const int j = i - 16;
            return __builtin_amdgcn_raw_buffer_load_b128(rv, vvo, (j >> 3) * 8192 + ((j >> 2) & 1) * 4096 + (j & 3) * 256, AUX_NT);
        };
        KvOn on{L + KS_OFF + (w - 1) * 16384, lane};
        v4u r[32];
        __syncthreads();
        stream_head<0>(ld, r, FL + FL_PROG + prev);
        stream_step<WCH>(ld, on, r, FL + FL_PROG + w);
        STAMP(2);
        lds_wait_ge(FL + FL_Q, 2);
        STAMP(3);
        __builtin_amdgcn_s_setprio(3);
        v4u qf[4];
#pragma unroll
        for (int s = 0; s < 4; ++s) qf[s] = *(const LAS v4u*)(L + QS_OFF + (c & 7) * QS_STR + (32 * s + 8 * g) * 2);
        f32x4 S[4];
#pragma unroll
        for (int t = 0; t < 4; ++t) S[t] = f32x4{0.f, 0.f, 0.f, 0.f};
#pragma unroll
        for (int s = 0; s < 4; ++s)
#pragma unroll
            for (int t = 0; t < 4; ++t) S[t] = mfma(r[4 * t + s], qf[s], S[t]);
        float mx = fmaxf(fmaxf(fmaxf(S[0][0], S[0][1]), fmaxf(S[0][2], S[0][3])), fmaxf(fmaxf(S[1][0], S[1][1]), fmaxf(S[1][2], S[1][3])));
        mx = fmaxf(mx, fmaxf(fmaxf(fmaxf(S[2][0], S[2][1]), fmaxf(S[2][2], S[2][3])), fmaxf(fmaxf(S[3][0], S[3][1]), fmaxf(S[3][2], S[3][3]))));
        mx = xrow16_max(mx);
        LAS float* st1 = (LAS float*)(L + ST1_OFF);
        f32x4 O[4];
        float lsum = 0.f, m = mx;
        if (w == 2) {
            lds_wait_ge(FL + FL_KV1, 1);
            const float m1 = st1[c & 7], l1 = st1[8 + (c & 7)];
            m = fmaxf(mx, m1);
            const float f1 = __builtin_amdgcn_exp2f((m1 - m) * C2);
            lsum = (g == 0) ? f1 * l1 : 0.f;
            if (lane < 8) st1[16 + lane] = f1;
            CBAR();
            const f32x4 f4 = *(const LAS f32x4*)(st1 + 16 + 4 * (g & 1));
#pragma unroll
            for (int e = 0; e < 4; ++e) O[e] = *(const LAS f32x4*)(L + KV_OFF + (e * 64 + lane) * 16) * f4;
        } else {
#pragma unroll
            for (int e = 0; e < 4; ++e) O[e] = f32x4{0.f, 0.f, 0.f, 0.f};
        }
        const float mc = m * C2;
        float ls4[4];
#pragma unroll
        for (int t = 0; t < 4; ++t) {
#pragma unroll
            for (int q = 0; q < 4; ++q) S[t][q] = __builtin_amdgcn_exp2f(fmaf(S[t][q], C2, -mc));
            ls4[t] = (S[t][0] + S[t][1]) + (S[t][2] + S[t][3]);
        }
        lsum += (ls4[0] + ls4[1]) + (ls4[2] + ls4[3]);
        const bool lo8 = c < 8;
#pragma unroll
        for (int u = 0; u < 2; ++u) {
            const v4u pa = {pk_bf16(S[2 * u][0], S[2 * u][1]), pk_bf16(S[2 * u][2], S[2 * u][3]),
                            pk_bf16(S[2 * u + 1][0], S[2 * u + 1][1]), pk_bf16(S[2 * u + 1][2], S[2 * u + 1][3])};
            const v4u z = {0u, 0u, 0u, 0u};
            const v4u p0 = lo8 ? pa : z, p1 = lo8 ? z : pa;
#pragma unroll
            for (int e = 0; e < 4; ++e) O[e] = mfma(p0, r[16 + 8 * u + e], O[e]);
#pragma unroll
            for (int e = 0; e < 4; ++e) O[e] = mfma(p1, r[16 + 8 * u + 4 + e], O[e]);
        }
        lsum = xrow16_sum(lsum);
        if (w == 1) {
#pragma unroll
            for (int e = 0; e < 4; ++e) *(LAS f32x4*)(L + KV_OFF + (e * 64 + lane) * 16) = O[e];
            if (lane < 8) { st1[lane] = m; st1[8 + lane] = lsum; }
            lds_set(FL + FL_KV1, 1);
            STAMP(4);
        } else {
            const unsigned tag = __builtin_amdgcn_readfirstlane(FL[FL_TV]);
            const int obase = OP_OFF + ((gid * 8 + sp) * 8 + 4 * (g & 1)) * 512 + (g >> 1) * 256 + c * 16;
#pragma unroll
            for (int rr = 0; rr < 4; ++rr)
                __builtin_amdgcn_raw_buffer_store_b128(v4u{pk_bf16(O[0][rr], O[1][rr]), tag, pk_bf16(O[2][rr], O[3][rr]), tag}, rs, obase, rr * 512, AUX_SC1);
            if (lane < 8) __builtin_amdgcn_raw_buffer_store_b128(v4u{f2u(m), tag, f2u(lsum), tag}, rs, OM_OFF + ((gid * 8 + lane) * 8 + sp) * 16, 0, AUX_SC1);
            lds_set(FL + FL_OT, 1);
            STAMP(4);
        }
    } else {
        const int hw = w - 6;
        unsigned ep = 0;
        if (hw == 1) ep = __hip_atomic_load((GAS unsigned*)(p.ws + WS_EPOCH), __ATOMIC_RELAXED, __HIP_MEMORY_SCOPE_AGENT);
        {
            v4u xv[4];
            load_x_rows(p.x, 4 + hw * 2, lane, xv);
            __syncthreads();
            if (hw == 1) {
                FL[FL_TV] = __builtin_amdgcn_readfirstlane(ep) + 1u;
                lds_set(FL + FL_T, 1);
            }
            ln_rows(xv, L + XN_OFF + (4 + hw * 2) * XN_STR, lane);
            lds_inc(FL + FL_XN, lane);
            STAMP(8);
        }
        __builtin_amdgcn_s_setprio(3);
        lds_wait_ge(FL + FL_T, 1);
        const unsigned tag = __builtin_amdgcn_readfirstlane(FL[FL_TV]);
        {
            const int gid = xcd * 4 + (idx >> 3), bq = gid >> 2, hk = gid & 3;
            int vo[8];
#pragma unroll
            for (int i = 0; i < 8; ++i) vo[i] = QP_OFF + ((i >> 1) * 8 + bq) * QP_ROW + (hk * 16 + hw * 8 + (i & 1) * 4 + g) * 256 + c * 16;
            lds_wait_ge(FL + FL_QT, 1);
            __builtin_amdgcn_s_sleep(MK_DQ);
            v4u q8[8];
            gather<8>(rs, vo, tag, q8);
            STAMP(3);
            const int n4 = 8 * (c & 7) + 4 * (c >> 3);
#pragma unroll
            for (int sh = 0; sh < 2; ++sh) {
                const float v0 = (bf_lo(q8[sh].x) + bf_lo(q8[2 + sh].x)) + (bf_lo(q8[4 + sh].x) + bf_lo(q8[6 + sh].x));
                const float v1 = (bf_hi(q8[sh].x) + bf_hi(q8[2 + sh].x)) + (bf_hi(q8[4 + sh].x) + bf_hi(q8[6 + sh].x));
                const float v2 = (bf_lo(q8[sh].z) + bf_lo(q8[2 + sh].z)) + (bf_lo(q8[4 + sh].z) + bf_lo(q8[6 + sh].z));
                const float v3 = (bf_hi(q8[sh].z) + bf_hi(q8[2 + sh].z)) + (bf_hi(q8[4 + sh].z) + bf_hi(q8[6 + sh].z));
                const int n = (hw * 8 + sh * 4 + g) * 64 + n4;
                *(LAS v2u*)(L + QS_OFF + (n >> 7) * QS_STR + (n & 127) * 2) = v2u{pk_bf16(v0, v1), pk_bf16(v2, v3)};
            }
            lds_inc(FL + FL_Q, lane);
            STAMP(4);
        }
        {
            const int head = ks_o >> 1, dh = ks_o & 1, hk = head >> 3, hh = head & 7;
            const int gidb = (4 * hw + g) * 4 + hk;
            int vo[9];
#pragma unroll
            for (int i = 0; i < 8; ++i) vo[i] = OP_OFF + ((gidb * 8 + i) * 8 + hh) * 512 + (c >> 3) * 256 + (8 * dh + (c & 7)) * 16;
            vo[8] = OM_OFF + ((gidb * 8 + hh) * 8 + (c & 7)) * 16;
            lds_wait_ge(FL + FL_OT, 1);
            __builtin_amdgcn_s_sleep(MK_DO);
            v4u o9[9];
            gather<9>(rs, vo, tag, o9);
            STAMP(5);
            const float ms = u2f(o9[8].x), ls = u2f(o9[8].z);
            float M = ms;
            M = fmaxf(M, dpp<XOR1>(M)); M = fmaxf(M, dpp<XOR2>(M)); M = fmaxf(M, dpp<XOR7>(M));
            const float we = __builtin_amdgcn_exp2f((ms - M) * C2);
            float Ls = we * ls;
            Ls += dpp<XOR1>(Ls); Ls += dpp<XOR2>(Ls); Ls += dpp<XOR7>(Ls);
            const float wn = we / Ls;
            LAS float* wl = (LAS float*)(L + ST1_OFF + 128) + hw * 32 + g * 8;
            if (c < 8) wl[c] = wn;
            CBAR();
            const f32x4 wa = *(const LAS f32x4*)wl, wb = *(const LAS f32x4*)(wl + 4);
            float v0 = 0.f, v1 = 0.f, v2 = 0.f, v3 = 0.f;
#pragma unroll
            for (int i = 0; i < 8; ++i) {
                const float wi = i < 4 ? wa[i & 3] : wb[i & 3];
                v0 = fmaf(wi, bf_lo(o9[i].x), v0); v1 = fmaf(wi, bf_hi(o9[i].x), v1);
                v2 = fmaf(wi, bf_lo(o9[i].z), v2); v3 = fmaf(wi, bf_hi(o9[i].z), v3);
            }
            *(LAS v2u*)(L + OA_OFF + (4 * hw + g) * AC_STR + (8 * (c & 7) + 4 * (c >> 3)) * 2) = v2u{pk_bf16(v0, v1), pk_bf16(v2, v3)};
            lds_inc(FL + FL_O, lane);
            STAMP(6);
        }
        if (hw == 1) {
            const int nsl = bid & 3, j = bid >> 2, st = j >> 3, G = (j & 7) >> 1, lam = lane & 7, cc = 8 * (j & 1) + lam, src = lane >> 3;
            const int b = 4 * (G & 1) + (st >> 1), n = 256 * nsl + 128 * (G >> 1) + 8 * cc + 4 * (st & 1);
            const v2u xx = *(const GAS v2u*)(p.x + b * D + n), bb = *(const GAS v2u*)(p.b2 + n);
            const int uo = (st * 64 + 8 * (j & 7) + lam) * 16;
            float sv[4], yv[4];
            {
                int vo[8];
#pragma unroll
                for (int i = 0; i < 8; ++i) vo[i] = SP_OFF + (nsl * 64 + 8 * i + src) * 8192 + uo;
                lds_wait_ge(FL + FL_ST, 1);
                __builtin_amdgcn_s_sleep(MK_DF);
                v4u f8[8];
                gather<8>(rs, vo, tag, f8);
                sv[0] = sv[1] = sv[2] = sv[3] = 0.f;
#pragma unroll
                for (int i = 0; i < 8; ++i) { sv[0] += bf_lo(f8[i].x); sv[1] += bf_hi(f8[i].x); sv[2] += bf_lo(f8[i].z); sv[3] += bf_hi(f8[i].z); }
#pragma unroll
                for (int q = 0; q < 4; ++q) { sv[q] += dpp<XOR8>(sv[q]); sv[q] = xrow16_sum(sv[q]); }
            }
            STAMP(7);
            {
                int vo[7];
#pragma unroll
                for (int i = 0; i < 7; ++i) vo[i] = YP_OFF + (nsl * 56 + 8 * i + src) * 8192 + uo;
                lds_wait_ge(FL + FL_YT, 1);
                __builtin_amdgcn_s_sleep(MK_DF);
                v4u f7[7];
                gather<7>(rs, vo, tag, f7);
                yv[0] = yv[1] = yv[2] = yv[3] = 0.f;
#pragma unroll
                for (int i = 0; i < 7; ++i) { yv[0] += bf_lo(f7[i].x); yv[1] += bf_hi(f7[i].x); yv[2] += bf_lo(f7[i].z); yv[3] += bf_hi(f7[i].z); }
#pragma unroll
                for (int q = 0; q < 4; ++q) { yv[q] += dpp<XOR8>(yv[q]); yv[q] = xrow16_sum(yv[q]); }
            }
            const float o0 = bf_lo(xx.x) + sv[0] * silu(yv[0] + bf_lo(bb.x)), o1 = bf_hi(xx.x) + sv[1] * silu(yv[1] + bf_hi(bb.x));
            const float o2 = bf_lo(xx.y) + sv[2] * silu(yv[2] + bf_lo(bb.y)), o3 = bf_hi(xx.y) + sv[3] * silu(yv[3] + bf_hi(bb.y));
            if (lane < 8) *(GAS v2u*)(p.out + b * D + n) = v2u{pk_bf16(o0, o1), pk_bf16(o2, o3)};
            if (bid == 0 && lane == 0) __hip_atomic_store((GAS unsigned*)(p.ws + WS_EPOCH), tag, __ATOMIC_RELAXED, __HIP_MEMORY_SCOPE_AGENT);
            STAMP(1);
        }
    }
}

}

extern "C" void kernel_launch(void* const* d_in, const int* in_sizes, int n_in, void* d_out, int out_size, void* d_ws, size_t ws_size,
                              hipStream_t stream) {
    mk::Params p;
    p.x = (const u16*)d_in[0];
    p.kc = (const u16*)d_in[1];
    p.vc = (const u16*)d_in[2];
    p.wq = (const u16*)d_in[3];
    p.wo = (const u16*)d_in[4];
    p.w1 = (const u16*)d_in[6];
    p.b1 = (const u16*)d_in[7];
    p.w2 = (const u16*)d_in[8];
    p.b2 = (const u16*)d_in[9];
    p.out = (u16*)d_out;
    p.ws = (unsigned char*)d_ws;
    mk::fused_kernel<<<256, 512, 0, stream>>>(p);
}
```

```cpp
#include <hip/hip_runtime.h>
#include <hip/hip_bf16.h>
#include <stdint.h>

#define GAS __attribute__((address_space(1)))
#define LAS __attribute__((address_space(3)))
typedef unsigned short u16;
typedef unsigned v4u __attribute__((ext_vector_type(4)));
typedef unsigned v2u __attribute__((ext_vector_type(2)));
typedef float f32x4 __attribute__((ext_vector_type(4)));
typedef float f32x2 __attribute__((ext_vector_type(2)));
typedef __bf16 bf16x8 __attribute__((ext_vector_type(8)));
typedef __bf16 bf16x2 __attribute__((ext_vector_type(2)));

#define MK_TOUCH 64
#define MK_WCH 8
#define MK_STAMPS 0
#define MK_ORDER 0
#define MK_CSLEEP 0
#define MK_DQ 16
#define MK_DO 16
#define MK_DH 16
#define MK_DF 8

namespace mk {

constexpr int B = 8, D = 1024, SK = 4096, DH = 128, WIN = 1024, F = 3584, NQ = 4096;
constexpr int NCH = 8, CH = 4, WCH = MK_WCH;

constexpr int WS_EPOCH = 0;
constexpr int QP_ROW = 16384, QP_OFF = 4096;
constexpr int HP_ROW = 14336, HP_OFF = QP_OFF + 32 * QP_ROW;
constexpr int OP_OFF = HP_OFF + 32 * HP_ROW;
constexpr int OM_OFF = OP_OFF + 32 * 8 * 8 * 512;
constexpr int SP_OFF = OM_OFF + 32 * 8 * 8 * 16;
constexpr int YP_OFF = SP_OFF + 4 * 64 * 8192;
constexpr int WS_END = YP_OFF + 4 * 56 * 8192;
constexpr int ST_OFF = WS_END;
constexpr int WS_BYTES = ST_OFF + 256 * 8 * 16 * 8;

constexpr int XN_STR = 2048 + 16, XN_OFF = 0;
constexpr int QS_STR = 256 + 16, QS_OFF = XN_OFF + 8 * XN_STR;
constexpr int AC_STR = 128 + 16, OA_OFF = QS_OFF + 8 * QS_STR;
constexpr int HA_OFF = OA_OFF + 8 * AC_STR;
constexpr int ZR_OFF = HA_OFF + 8 * AC_STR;
constexpr int KV_OFF = ZR_OFF + 128;
constexpr int ST1_OFF = KV_OFF + 4096;
constexpr int FL_OFF = ST1_OFF + 512;
constexpr int KS_OFF = FL_OFF + 256;
constexpr int LDS_USED = KS_OFF + 32768;
constexpr int LDS_BYTES = 96 * 1024;
static_assert(LDS_USED + 1024 <= LDS_BYTES && (KS_OFF % 16) == 0 && (KV_OFF % 16) == 0 && (ZR_OFF % 16) == 0, "lds");
enum { FL_PROG = 0  , FL_XN = 8, FL_Q = 9, FL_KV1 = 10, FL_O = 11, FL_H = 12, FL_T = 13, FL_TV = 14, FL_HT = 15, FL_OT = 16, FL_ST = 17, FL_QT = 18, FL_YT = 19 };

struct Params {
    const u16 *x, *kc, *vc, *wq, *wo, *w1, *b1, *w2, *b2;
    u16* out;
    unsigned char* ws;
};

constexpr unsigned kSpinCap = 1u << 16;
constexpr int AUX_NT = 2, AUX_SC1 = 16;

__device__ __forceinline__ float bf_lo(unsigned u) { return __builtin_bit_cast(float, u << 16); }
__device__ __forceinline__ float bf_hi(unsigned u) { return __builtin_bit_cast(float, u & 0xffff0000u); }
__device__ __forceinline__ unsigned pk_bf16(float lo, float hi) {
    f32x2 v = {lo, hi};
    return __builtin_bit_cast(unsigned, __builtin_convertvector(v, bf16x2));
}
__device__ __forceinline__ f32x4 mfma(v4u a, v4u b, f32x4 c) {
    return __builtin_amdgcn_mfma_f32_16x16x32_bf16(__builtin_bit_cast(bf16x8, a), __builtin_bit_cast(bf16x8, b), c, 0, 0, 0);
}
__device__ __forceinline__ unsigned f2u(float f) { return __builtin_bit_cast(unsigned, f); }
__device__ __forceinline__ float u2f(unsigned u) { return __builtin_bit_cast(float, u); }

#define CBAR() asm volatile("" ::: "memory")
template <int N> __device__ __forceinline__ void waitvm() { asm volatile("s_waitcnt vmcnt(%0)" ::"i"(N) : "memory"); }

__device__ __forceinline__ void lds_set(LAS unsigned* p, unsigned v) {
    CBAR();
    __hip_atomic_store(p, v, __ATOMIC_RELAXED, __HIP_MEMORY_SCOPE_WORKGROUP);
    CBAR();
}
__device__ __forceinline__ void lds_inc(LAS unsigned* p, int lane) {
    CBAR();
    if (lane == 0) __hip_atomic_fetch_add(p, 1u, __ATOMIC_RELAXED, __HIP_MEMORY_SCOPE_WORKGROUP);
    CBAR();
}
__device__ __forceinline__ unsigned lds_get(LAS unsigned* p) {
    return (unsigned)__builtin_amdgcn_readfirstlane(__hip_atomic_load(p, __ATOMIC_RELAXED, __HIP_MEMORY_SCOPE_WORKGROUP));
}
__device__ __forceinline__ void lds_wait_ge(LAS unsigned* p, unsigned v) {
#pragma nounroll
    for (unsigned spins = 0; spins < kSpinCap; ++spins) {
        if (lds_get(p) >= v) break;
        __builtin_amdgcn_s_sleep(1);
    }
    CBAR();
}

__device__ __forceinline__ float xrow16_max(float x) {
    auto s = __builtin_amdgcn_permlane16_swap(f2u(x), f2u(x), false, false);
    x = fmaxf(u2f(s[0]), u2f(s[1]));
    auto t = __builtin_amdgcn_permlane32_swap(f2u(x), f2u(x), false, false);
    return fmaxf(u2f(t[0]), u2f(t[1]));
}
__device__ __forceinline__ float xrow16_sum(float x) {
    auto s = __builtin_amdgcn_permlane16_swap(f2u(x), f2u(x), false, false);
    x = u2f(s[0]) + u2f(s[1]);
    auto t = __builtin_amdgcn_permlane32_swap(f2u(x), f2u(x), false, false);
    return u2f(t[0]) + u2f(t[1]);
}
template <int CTRL> __device__ __forceinline__ float dpp(float x) {
    return u2f(__builtin_amdgcn_mov_dpp(f2u(x), CTRL, 0xf, 0xf, true));
}
constexpr int XOR1 = 0xB1, XOR2 = 0x4E, XOR7 = 0x141, XOR8 = 0x128;
__device__ __forceinline__ float row16_sum(float x) {
    x += dpp<XOR1>(x); x += dpp<XOR2>(x); x += dpp<XOR7>(x); x += dpp<XOR8>(x);
    return x;
}
__device__ __forceinline__ float wave_sum(float x) { return xrow16_sum(row16_sum(x)); }

#define STAMP(i) do {} while (0)

__device__ __forceinline__ void transpose8(v4u* r) {
    v4u o[8];
#pragma unroll
    for (int t = 0; t < 4; ++t)
#pragma unroll
        for (int jp = 0; jp < 4; ++jp) {
            const unsigned x0 = r[2 * jp][t], x1 = r[2 * jp + 1][t];
            o[2 * t][jp] = __builtin_amdgcn_perm(x1, x0, 0x05040100u);
            o[2 * t + 1][jp] = __builtin_amdgcn_perm(x1, x0, 0x07060302u);
        }
#pragma unroll
    for (int e = 0; e < 8; ++e) { asm volatile("" : "+v"(o[e])); r[e] = o[e]; }
}

__device__ __forceinline__ void lds_wait_ge_fast(LAS unsigned* p, unsigned v) {
#pragma nounroll
    for (unsigned spins = 0; spins < 16u * kSpinCap; ++spins) {
        if (lds_get(p) >= v) break;
    }
    CBAR();
}
template <int K, class LD>
__device__ __forceinline__ void stream_head(LD& ld, v4u (&r)[32], LAS unsigned* prev) {
    if constexpr (K == 0) lds_wait_ge(prev, NCH + K - WCH > 0 ? NCH + K - WCH : 1);
    lds_wait_ge_fast(prev, NCH + K - WCH + 1);
#pragma unroll
    for (int i = 0; i < CH; ++i) r[K * CH + i] = ld(K * CH + i);
    __builtin_amdgcn_sched_barrier(0);
    if constexpr (K + 1 < WCH) stream_head<K + 1>(ld, r, prev);
}
template <int K, class LD, class ON>
__device__ __forceinline__ void stream_step(LD& ld, ON& on, v4u (&r)[32], LAS unsigned* me) {
    if constexpr (K < NCH) {
        waitvm<(WCH - 1) * CH>();
        lds_set(me, K - WCH + 1);
#pragma unroll
        for (int i = 0; i < CH; ++i) r[K * CH + i] = ld(K * CH + i);
        __builtin_amdgcn_sched_barrier(0);
        on.template run<K - WCH>(r);
        __builtin_amdgcn_sched_barrier(0);
    } else {
        waitvm<(NCH + WCH - 1 - K) * CH>();
        lds_set(me, K - WCH + 1);
        on.template run<K - WCH>(r);
        __builtin_amdgcn_sched_barrier(0);
    }
    if constexpr (K + 1 < NCH + WCH) stream_step<K + 1>(ld, on, r, me);
}

template <int N>
__device__ __forceinline__ void gather(__amdgpu_buffer_rsrc_t rs, const int (&voff)[N], unsigned tag, v4u (&g)[N]) {
    unsigned pend = (1u << N) - 1u;
#pragma nounroll
    for (unsigned spins = 0; spins < kSpinCap; ++spins) {
        CBAR();
#pragma unroll
        for (int i = 0; i < N; ++i)
            if (pend & (1u << i)) g[i] = __builtin_amdgcn_raw_buffer_load_b128(rs, voff[i], 0, AUX_SC1);
#pragma unroll
        for (int i = 0; i < N; ++i)
            if ((pend & (1u << i)) && __all(g[i].y == tag && g[i].w == tag)) pend &= ~(1u << i);
        if (pend == 0u) break;
    }
    CBAR();
}

struct GemmOn {
    LAS unsigned* flag; unsigned need;
    const LAS unsigned char* pa0; const LAS unsigned char* pa1; int step;
    f32x4 (&acc)[8];
    v4u (&a)[4];
    int done;
    __device__ __forceinline__ void load_a() {
        a[0] = *(const LAS v4u*)(pa0); a[1] = *(const LAS v4u*)(pa1);
        a[2] = *(const LAS v4u*)(pa0 + step); a[3] = *(const LAS v4u*)(pa1 + step);
    }
    template <int G> __device__ __forceinline__ void mul(v4u (&r)[32]) {
#pragma unroll
        for (int e = 0; e < 8; ++e) acc[e] = mfma(a[G], r[8 * G + e], acc[e]);
    }
    template <int C> __device__ __forceinline__ void run(v4u (&r)[32]) {
        if constexpr (C & 1) {
            constexpr int G = C >> 1;
            transpose8(&r[8 * G]);
            if (done < 0 && lds_get(flag) >= need) {
                CBAR();
                load_a();
                done = 0;
                if constexpr (G >= 1) mul<0>(r);
                if constexpr (G >= 2) mul<1>(r);
                if constexpr (G >= 3) mul<2>(r);
            }
            if (done >= 0) mul<G>(r);
        }
    }
    __device__ __forceinline__ void finish(v4u (&r)[32]) {
        if (done < 0) {
            lds_wait_ge(flag, need);
            load_a();
            mul<0>(r); mul<1>(r); mul<2>(r); mul<3>(r);
        }
    }
};
struct KvOn {
    LAS unsigned char* ks; int lane;
    template <int C> __device__ __forceinline__ void run(v4u (&r)[32]) {
        if constexpr (C == 1 || C == 3) {
            constexpr int H = C >> 1;
            const int g = lane >> 4, c = lane & 15;
#pragma unroll
            for (int i = 0; i < 8; ++i) {
                const int key = 32 * H + 4 * i + g;
                *(LAS v4u*)(ks + key * 256 + ((c ^ ((4 * i + g) & 15)) * 16)) = r[8 * H + i];
            }
            CBAR();
#pragma unroll
            for (int t = 0; t < 2; ++t)
#pragma unroll
                for (int s = 0; s < 4; ++s) r[8 * H + 4 * t + s] = *(const LAS v4u*)(ks + (32 * H + 16 * t + c) * 256 + (((4 * s + g) ^ c) * 16));
        }
        if constexpr (C == 5) transpose8(&r[16]);
        if constexpr (C == 7) transpose8(&r[24]);
    }
};

__device__ __forceinline__ void fold_halves(const f32x4 (&acc)[8], int lane, float (&res)[8]) {
    const bool lo = (lane & 8) == 0;
#pragma unroll
    for (int e = 0; e < 8; ++e) {
        auto s = __builtin_amdgcn_permlane32_swap(f2u(acc[e][0]), f2u(acc[e][1]), false, false);
        auto t = __builtin_amdgcn_permlane32_swap(f2u(acc[e][2]), f2u(acc[e][3]), false, false);
        float s1 = u2f(s[0]) + dpp<XOR8>(u2f(s[1]));
        float s2 = u2f(t[1]) + dpp<XOR8>(u2f(t[0]));
        asm volatile("" : "+v"(s1), "+v"(s2));
        res[e] = lo ? s1 : s2;
    }
}

__device__ __forceinline__ void ln_rows(const v4u (&xv)[4], LAS unsigned char* dst, int lane) {
    float v[2][16], mean[2], rstd[2];
#pragma unroll
    for (int k = 0; k < 2; ++k) {
#pragma unroll
        for (int q = 0; q < 4; ++q) {
            v[k][2 * q] = bf_lo(xv[2 * k][q]); v[k][2 * q + 1] = bf_hi(xv[2 * k][q]);
            v[k][8 + 2 * q] = bf_lo(xv[2 * k + 1][q]); v[k][8 + 2 * q + 1] = bf_hi(xv[2 * k + 1][q]);
        }
        float s4[4];
#pragma unroll
        for (int q = 0; q < 4; ++q) s4[q] = (v[k][4 * q] + v[k][4 * q + 1]) + (v[k][4 * q + 2] + v[k][4 * q + 3]);
        mean[k] = (s4[0] + s4[1]) + (s4[2] + s4[3]);
    }
    mean[0] = wave_sum(mean[0]) * (1.0f / D);
    mean[1] = wave_sum(mean[1]) * (1.0f / D);
#pragma unroll
    for (int k = 0; k < 2; ++k) {
#pragma unroll
        for (int i = 0; i < 16; ++i) v[k][i] -= mean[k];
        float s4[4];
#pragma unroll
        for (int q = 0; q < 4; ++q)
            s4[q] = fmaf(v[k][4 * q], v[k][4 * q], v[k][4 * q + 1] * v[k][4 * q + 1]) + fmaf(v[k][4 * q + 2], v[k][4 * q + 2], v[k][4 * q + 3] * v[k][4 * q + 3]);
        rstd[k] = (s4[0] + s4[1]) + (s4[2] + s4[3]);
    }
    rstd[0] = wave_sum(rstd[0]);
    rstd[1] = wave_sum(rstd[1]);
#pragma unroll
    for (int k = 0; k < 2; ++k) {
        const float rs_ = 1.0f / sqrtf(rstd[k] * (1.0f / D) + 1e-5f);
#pragma unroll
        for (int h = 0; h < 2; ++h) {
            const v4u o = {pk_bf16(v[k][h * 8] * rs_, v[k][h * 8 + 1] * rs_), pk_bf16(v[k][h * 8 + 2] * rs_, v[k][h * 8 + 3] * rs_),
                           pk_bf16(v[k][h * 8 + 4] * rs_, v[k][h * 8 + 5] * rs_), pk_bf16(v[k][h * 8 + 6] * rs_, v[k][h * 8 + 7] * rs_)};
            *(LAS v4u*)(dst + k * XN_STR + h * 1024 + lane * 16) = o;
        }
    }
}
__device__ __forceinline__ void load_x_rows(const u16* x, int row0, int lane, v4u (&xv)[4]) {
    const GAS unsigned char* xp = (const GAS unsigned char*)x + row0 * 2048 + lane * 16;
#pragma unroll
    for (int i = 0; i < 4; ++i) xv[i] = *(const GAS v4u*)(xp + i * 1024);
}

__device__ __forceinline__ float gelu_tanh(float s) {
    const float u = 0.7978845608028654f * (s + 0.044715f * s * s * s);
    const float e = __expf(2.0f * u);
    const float th = 1.0f - 2.0f / (e + 1.0f);
    return 0.5f * s * (1.0f + th);
}
__device__ __forceinline__ float silu(float y) { return y / (1.0f + __expf(-y)); }

__global__ void __launch_bounds__(512, 2) fused_kernel(Params p) {
    __shared__ __attribute__((aligned(16))) unsigned char smem[LDS_BYTES];
    LAS unsigned char* L = (LAS unsigned char*)smem;
    LAS unsigned* FL = (LAS unsigned*)(L + FL_OFF);
    const int tid = threadIdx.x, lane = tid & 63, w = __builtin_amdgcn_readfirstlane(tid >> 6), bid = blockIdx.x;
    const int g = lane >> 4, c = lane & 15;
    const __amdgpu_buffer_rsrc_t rs = __builtin_amdgcn_make_buffer_rsrc((void*)p.ws, 0, WS_BYTES, 0x00020000);
    STAMP(0);
    constexpr float C2 = 0.08838834764831845f * 1.4426950408889634f;

    const int xcd = bid & 7, idx = bid >> 3;
    const bool has_w1 = bid < 224;
    const bool has_w2 = idx < 28;
    const int ks_o = xcd * 8 + (idx >> 2), nsl_o = idx & 3;
    const int t2 = xcd * 28 + idx, ks_2 = t2 >> 2, nsl_2 = t2 & 3;
    int prev;
    if (MK_ORDER == 0) prev = (w == 4 && !has_w1) ? 2 : w - 1;
    else prev = w == 3 ? 0 : w == 1 ? (has_w1 ? 3 : 0) : w == 5 ? 2 : w == 4 ? (has_w2 ? 5 : 2) : w - 1;

    const bool is_gemm = (w == 0 || w == 3 || w == 4 || w == 5);
    if (is_gemm) {
        v4u r[32];
        const bool ta = (w == 0 || w == 3);
        const u16* W = w == 0 ? p.wq : w == 3 ? p.w1 : w == 4 ? p.wo : p.w2;
        const int ldw = w == 0 ? NQ * 2 : w == 3 ? F * 2 : D * 2;
        const int nbytes = w == 3 || w == 5 ? D * F * 2 : D * NQ * 2;
        const int ks = ta ? (bid & 3) : w == 4 ? ks_o : ks_2;
        const int ns = ta ? (bid >> 2) : w == 4 ? nsl_o : nsl_2;
        const __amdgpu_buffer_rsrc_t rw = __builtin_amdgcn_make_buffer_rsrc((void*)W, 0, nbytes, 0x00020000);
        const int voffw = ta ? (ks * 256 + 32 * (c >> 3) + 8 * g) * ldw + (ns * 64 + 8 * (c & 7)) * 2
                             : (ks * 64 + 8 * g) * ldw + (ns * 256 + 8 * c) * 2;
        const int go1 = ta ? 64 * ldw : 256, go2 = ta ? 128 * ldw : 32 * ldw;
        auto ldg = [&](int i) {
            return __builtin_amdgcn_raw_buffer_load_b128(rw, voffw, (i & 7) * ldw + ((i >> 3) & 1) * go1 + (i >> 4) * go2, AUX_NT);
        };
        if (w == 0) {
            FL[lane] = 0u;
            if (lane < 32) ((LAS unsigned*)(L + ZR_OFF))[lane] = 0u;
#pragma unroll
            for (int i = 0; i < WCH * CH; ++i) r[i] = ldg(i);
            __builtin_amdgcn_sched_barrier(0);
            __syncthreads();
        } else {
            v4u xv[4];
            if (w >= 4) load_x_rows(p.x, (w - 4) * 2, lane, xv);
            __syncthreads();
            if (w >= 4) { ln_rows(xv, L + XN_OFF + (w - 4) * 2 * XN_STR, lane); lds_inc(FL + FL_XN, lane); STAMP(8); }
        }
        const bool has = w == 3 ? has_w1 : w == 5 ? has_w2 : true;
        LAS unsigned* trig = FL + (w == 0 ? FL_QT : w == 3 ? FL_HT : w == 4 ? FL_ST : FL_YT);
        if (has) {
            unsigned bbias = 0;
            if (w != 0) stream_head<0>(ldg, r, FL + FL_PROG + prev);
            f32x4 acc[8];
#pragma unroll
            for (int e = 0; e < 8; ++e) acc[e] = f32x4{0.f, 0.f, 0.f, 0.f};
            v4u a[4];
            const LAS unsigned char* pa0; const LAS unsigned char* pa1; int step;
            if (ta) {
                pa0 = L + XN_OFF + ks * 512 + (c & 7) * XN_STR + (32 * (c >> 3) + 8 * g) * 2; pa1 = pa0 + 128; step = 256;
            } else {
                const LAS unsigned char* act = L + (w == 4 ? OA_OFF : HA_OFF) + (c & 7) * AC_STR + g * 16;
                const LAS unsigned char* zr = L + ZR_OFF + g * 16;
                pa0 = c < 8 ? act : zr; pa1 = c < 8 ? zr : act; step = 64;
            }
            GemmOn on{FL + (ta ? FL_XN : w == 4 ? FL_O : FL_H), ta ? 4u : w == 4 ? 2u : 1u, pa0, pa1, step, acc, a, -1};
            stream_step<WCH>(ldg, on, r, FL + FL_PROG + w);
            STAMP(2);
            on.finish(r);
            STAMP(3);
            lds_wait_ge(FL + FL_T, 1);
            const unsigned tag = __builtin_amdgcn_readfirstlane(FL[FL_TV]);
            __builtin_amdgcn_s_setprio(3);
            if (ta) {
                float res[8];
                fold_halves(acc, lane, res);
                const int b = 4 * (g & 1) + (g >> 1) + 2 * (c >> 3);
                const int o = (w == 0 ? QP_OFF + (ks * 8 + b) * QP_ROW : HP_OFF + (ks * 8 + b) * HP_ROW) + ns * 256 + (c & 7) * 16;
#pragma unroll
                for (int u = 0; u < 2; ++u)
                    __builtin_amdgcn_raw_buffer_store_b128(v4u{pk_bf16(res[4 * u], res[4 * u + 1]), tag, pk_bf16(res[4 * u + 2], res[4 * u + 3]), tag}, rs, o, u * 128, AUX_SC1);
            } else {
                const int o = (w == 4 ? SP_OFF + (ns * 64 + ks) * 8192 : YP_OFF + (ns * 56 + ks) * 8192) + lane * 16;
#pragma unroll
                for (int rr = 0; rr < 4; ++rr)
#pragma unroll
                    for (int eh = 0; eh < 2; ++eh)
                        __builtin_amdgcn_raw_buffer_store_b128(
                            v4u{pk_bf16(acc[4 * eh][rr], acc[4 * eh + 1][rr]), tag, pk_bf16(acc[4 * eh + 2][rr], acc[4 * eh + 3][rr]), tag}, rs, o,
                            (2 * rr + eh) * 1024, AUX_SC1);
            }
            lds_set(trig, 1);
            STAMP(4);
            (void)bbias;
        } else {
            if (w == 3) lds_wait_ge(FL + FL_PROG + (MK_ORDER == 0 ? 4 : 1), NCH / 2);
            else lds_wait_ge(FL + FL_PROG + 4, NCH);
            lds_set(trig, 1);
        }
        if (w == 3 && has_w2) {
            lds_wait_ge(FL + FL_T, 1);
            const unsigned tag = __builtin_amdgcn_readfirstlane(FL[FL_TV]);
            const int n4 = 8 * (c & 7) + 4 * (c >> 3);
            const v2u bb = *(const GAS v2u*)(p.b1 + ks_2 * 64 + n4);
            int vo[8];
#pragma unroll
            for (int i = 0; i < 8; ++i) vo[i] = HP_OFF + ((i >> 1) * 8 + (i & 1) * 4 + g) * HP_ROW + ks_2 * 256 + c * 16;
            __builtin_amdgcn_s_sleep(MK_DH);
            v4u h8[8];
            gather<8>(rs, vo, tag, h8);
            STAMP(5);
#pragma unroll
            for (int bh = 0; bh < 2; ++bh) {
                float v0 = (bf_lo(h8[bh].x) + bf_lo(h8[2 + bh].x)) + (bf_lo(h8[4 + bh].x) + bf_lo(h8[6 + bh].x)) + bf_lo(bb.x);
                float v1 = (bf_hi(h8[bh].x) + bf_hi(h8[2 + bh].x)) + (bf_hi(h8[4 + bh].x) + bf_hi(h8[6 + bh].x)) + bf_hi(bb.x);
                float v2 = (bf_lo(h8[bh].z) + bf_lo(h8[2 + bh].z)) + (bf_lo(h8[4 + bh].z) + bf_lo(h8[6 + bh].z)) + bf_lo(bb.y);
                float v3 = (bf_hi(h8[bh].z) + bf_hi(h8[2 + bh].z)) + (bf_hi(h8[4 + bh].z) + bf_hi(h8[6 + bh].z)) + bf_hi(bb.y);
                *(LAS v2u*)(L + HA_OFF + (bh * 4 + g) * AC_STR + n4 * 2) = v2u{pk_bf16(gelu_tanh(v0), gelu_tanh(v1)), pk_bf16(gelu_tanh(v2), gelu_tanh(v3))};
            }
            lds_set(FL + FL_H, 1);
            STAMP(6);
        }
    } else if (w == 1 || w == 2) {
        const int gid = xcd * 4 + (idx >> 3), sp = idx & 7;
        const size_t kvbase = ((size_t)gid * SK + (SK - WIN) + sp * 128 + (w - 1) * 64) * (DH * 2);
        const __amdgpu_buffer_rsrc_t rk = __builtin_amdgcn_make_buffer_rsrc((void*)((const unsigned char*)p.kc + kvbase), 0, 64 * 256, 0x00020000);
        const __amdgpu_buffer_rsrc_t rv = __builtin_amdgcn_make_buffer_rsrc((void*)((const unsigned char*)p.vc + kvbase), 0, 64 * 256, 0x00020000);
        const int vvo = g * 1024 + c * 16;
        auto ld = [&](int i) {
            if (i < 16) return __builtin_amdgcn_raw_buffer_load_b128(rk, lane * 16, i * 1024, AUX_NT);
            const int j = i - 16;
            return __builtin_amdgcn_raw_buffer_load_b128(rv, vvo, (j >> 3) * 8192 + ((j >> 2) & 1) * 4096 + (j & 3) * 256, AUX_NT);
        };
        KvOn on{L + KS_OFF + (w - 1) * 16384, lane};
        v4u r[32];
        __syncthreads();
        stream_head<0>(ld, r, FL + FL_PROG + prev);
        stream_step<WCH>(ld, on, r, FL + FL_PROG + w);
        STAMP(2);
        lds_wait_ge(FL + FL_Q, 2);
        STAMP(3);
        __builtin_amdgcn_s_setprio(3);
        v4u qf[4];
#pragma unroll
        for (int s = 0; s < 4; ++s) qf[s] = *(const LAS v4u*)(L + QS_OFF + (c & 7) * QS_STR + (32 * s + 8 * g) * 2);
        f32x4 S[4];
#pragma unroll
        for (int t = 0; t < 4; ++t) S[t] = f32x4{0.f, 0.f, 0.f, 0.f};
#pragma unroll
        for (int s = 0; s < 4; ++s)
#pragma unroll
            for (int t = 0; t < 4; ++t) S[t] = mfma(r[4 * t + s], qf[s], S[t]);
        float mx = fmaxf(fmaxf(fmaxf(S[0][0], S[0][1]), fmaxf(S[0][2], S[0][3])), fmaxf(fmaxf(S[1][0], S[1][1]), fmaxf(S[1][2], S[1][3])));
        mx = fmaxf(mx, fmaxf(fmaxf(fmaxf(S[2][0], S[2][1]), fmaxf(S[2][2], S[2][3])), fmaxf(fmaxf(S[3][0], S[3][1]), fmaxf(S[3][2], S[3][3]))));
        mx = xrow16_max(mx);
        LAS float* st1 = (LAS float*)(L + ST1_OFF);
        f32x4 O[4];
        float lsum = 0.f, m = mx;
        if (w == 2) {
            lds_wait_ge(FL + FL_KV1, 1);
            const float m1 = st1[c & 7], l1 = st1[8 + (c & 7)];
            m = fmaxf(mx, m1);
            const float f1 = __builtin_amdgcn_exp2f((m1 - m) * C2);
            lsum = (g == 0) ? f1 * l1 : 0.f;
            if (lane < 8) st1[16 + lane] = f1;
            CBAR();
            const f32x4 f4 = *(const LAS f32x4*)(st1 + 16 + 4 * (g & 1));
#pragma unroll
            for (int e = 0; e < 4; ++e) O[e] = *(const LAS f32x4*)(L + KV_OFF + (e * 64 + lane) * 16) * f4;
        } else {
#pragma unroll
            for (int e = 0; e < 4; ++e) O[e] = f32x4{0.f, 0.f, 0.f, 0.f};
        }
        const float mc = m * C2;
        float ls4[4];
#pragma unroll
        for (int t = 0; t < 4; ++t) {
#pragma unroll
            for (int q = 0; q < 4; ++q) S[t][q] = __builtin_amdgcn_exp2f(fmaf(S[t][q], C2, -mc));
            ls4[t] = (S[t][0] + S[t][1]) + (S[t][2] + S[t][3]);
        }
        lsum += (ls4[0] + ls4[1]) + (ls4[2] + ls4[3]);
        const bool lo8 = c < 8;
#pragma unroll
        for (int u = 0; u < 2; ++u) {
            const v4u pa = {pk_bf16(S[2 * u][0], S[2 * u][1]), pk_bf16(S[2 * u][2], S[2 * u][3]),
                            pk_bf16(S[2 * u + 1][0], S[2 * u + 1][1]), pk_bf16(S[2 * u + 1][2], S[2 * u + 1][3])};
            const v4u z = {0u, 0u, 0u, 0u};
            const v4u p0 = lo8 ? pa : z, p1 = lo8 ? z : pa;
#pragma unroll
            for (int e = 0; e < 4; ++e) O[e] = mfma(p0, r[16 + 8 * u + e], O[e]);
#pragma unroll
            for (int e = 0; e < 4; ++e) O[e] = mfma(p1, r[16 + 8 * u + 4 + e], O[e]);
        }
        lsum = xrow16_sum(lsum);
        if (w == 1) {
#pragma unroll
            for (int e = 0; e < 4; ++e) *(LAS f32x4*)(L + KV_OFF + (e * 64 + lane) * 16) = O[e];
            if (lane < 8) { st1[lane] = m; st1[8 + lane] = lsum; }
            lds_set(FL + FL_KV1, 1);
            STAMP(4);
        } else {
            const unsigned tag = __builtin_amdgcn_readfirstlane(FL[FL_TV]);
            const int obase = OP_OFF + ((gid * 8 + sp) * 8 + 4 * (g & 1)) * 512 + (g >> 1) * 256 + c * 16;
#pragma unroll
            for (int rr = 0; rr < 4; ++rr)
                __builtin_amdgcn_raw_buffer_store_b128(v4u{pk_bf16(O[0][rr], O[1][rr]), tag, pk_bf16(O[2][rr], O[3][rr]), tag}, rs, obase, rr * 512, AUX_SC1);
            if (lane < 8) __builtin_amdgcn_raw_buffer_store_b128(v4u{f2u(m), tag, f2u(lsum), tag}, rs, OM_OFF + ((gid * 8 + lane) * 8 + sp) * 16, 0, AUX_SC1);
            lds_set(FL + FL_OT, 1);
            STAMP(4);
        }
    } else {
        const int hw = w - 6;
        unsigned ep = 0;
        if (hw == 1) ep = __hip_atomic_load((GAS unsigned*)(p.ws + WS_EPOCH), __ATOMIC_RELAXED, __HIP_MEMORY_SCOPE_AGENT);
        unsigned touch = 0;
        {
            v4u xv[4];
            load_x_rows(p.x, 4 + hw * 2, lane, xv);
            if (hw == 0) {
                touch = *(const GAS unsigned*)(p.ws + (size_t)lane * (MK_TOUCH * 1024) % WS_END);
                touch ^= *(const GAS unsigned*)(p.ws + (size_t)(64 + lane) * (MK_TOUCH * 1024) % WS_END);
            } else {
                const int gid = xcd * 4 + (idx >> 3), sp = idx & 7;
                const size_t kvb = ((size_t)gid * SK + (SK - WIN) + sp * 128) * (DH * 2);
                const unsigned char* a;
                if (lane < 8) a = (const unsigned char*)p.kc + kvb + lane * 4096;
                else if (lane < 16) a = (const unsigned char*)p.vc + kvb + (lane - 8) * 4096;
                else if (lane < 32) a = (const unsigned char*)p.w1 + ((size_t)((bid & 3) * 256 + (lane - 16) * 16) * F + (bid < 224 ? bid >> 2 : 0) * 64) * 2;
                else if (lane < 40) a = (const unsigned char*)p.wo + ((size_t)(ks_o * 64 + (lane - 32) * 8) * D + nsl_o * 256) * 2;
                else if (lane < 48) a = (const unsigned char*)p.w2 + ((size_t)((has_w2 ? ks_2 : 0) * 64 + (lane - 40) * 8) * D + nsl_2 * 256) * 2;
                else if (lane < 56) a = (const unsigned char*)p.out + (lane - 48) * 2048;
                else a = (const unsigned char*)p.b1 + (lane - 56) * 896;
                touch = *(const GAS unsigned*)a;
            }
            __syncthreads();
            if (hw == 1) {
                FL[FL_TV] = __builtin_amdgcn_readfirstlane(ep) + 1u;
                lds_set(FL + FL_T, 1);
            }
            ln_rows(xv, L + XN_OFF + (4 + hw * 2) * XN_STR, lane);
            lds_inc(FL + FL_XN, lane);
            STAMP(8);
            asm volatile("" :: "v"(touch));
        }
        __builtin_amdgcn_s_setprio(3);
        lds_wait_ge(FL + FL_T, 1);
        const unsigned tag = __builtin_amdgcn_readfirstlane(FL[FL_TV]);
        {
            const int gid = xcd * 4 + (idx >> 3), bq = gid >> 2, hk = gid & 3;
            int vo[8];
#pragma unroll
            for (int i = 0; i < 8; ++i) vo[i] = QP_OFF + ((i >> 1) * 8 + bq) * QP_ROW + (hk * 16 + hw * 8 + (i & 1) * 4 + g) * 256 + c * 16;
            lds_wait_ge(FL + FL_QT, 1);
            __builtin_amdgcn_s_sleep(MK_DQ);
            v4u q8[8];
            gather<8>(rs, vo, tag, q8);
            STAMP(3);
            const int n4 = 8 * (c & 7) + 4 * (c >> 3);
#pragma unroll
            for (int sh = 0; sh < 2; ++sh) {
                const float v0 = (bf_lo(q8[sh].x) + bf_lo(q8[2 + sh].x)) + (bf_lo(q8[4 + sh].x) + bf_lo(q8[6 + sh].x));
                const float v1 = (bf_hi(q8[sh].x) + bf_hi(q8[2 + sh].x)) + (bf_hi(q8[4 + sh].x) + bf_hi(q8[6 + sh].x));
                const float v2 = (bf_lo(q8[sh].z) + bf_lo(q8[2 + sh].z)) + (bf_lo(q8[4 + sh].z) + bf_lo(q8[6 + sh].z));
                const float v3 = (bf_hi(q8[sh].z) + bf_hi(q8[2 + sh].z)) + (bf_hi(q8[4 + sh].z) + bf_hi(q8[6 + sh].z));
                const int n = (hw * 8 + sh * 4 + g) * 64 + n4;
                *(LAS v2u*)(L + QS_OFF + (n >> 7) * QS_STR + (n & 127) * 2) = v2u{pk_bf16(v0, v1), pk_bf16(v2, v3)};
            }
            lds_inc(FL + FL_Q, lane);
            STAMP(4);
        }
        {
            const int head = ks_o >> 1, dh = ks_o & 1, hk = head >> 3, hh = head & 7;
            const int gidb = (4 * hw + g) * 4 + hk;
            int vo[9];
#pragma unroll
            for (int i = 0; i < 8; ++i) vo[i] = OP_OFF + ((gidb * 8 + i) * 8 + hh) * 512 + (c >> 3) * 256 + (8 * dh + (c & 7)) * 16;
            vo[8] = OM_OFF + ((gidb * 8 + hh) * 8 + (c & 7)) * 16;
            lds_wait_ge(FL + FL_OT, 1);
            __builtin_amdgcn_s_sleep(MK_DO);
            v4u o9[9];
            gather<9>(rs, vo, tag, o9);
            STAMP(5);
            const float ms = u2f(o9[8].x), ls = u2f(o9[8].z);
            float M = ms;
            M = fmaxf(M, dpp<XOR1>(M)); M = fmaxf(M, dpp<XOR2>(M)); M = fmaxf(M, dpp<XOR7>(M));
            const float we = __builtin_amdgcn_exp2f((ms - M) * C2);
            float Ls = we * ls;
            Ls += dpp<XOR1>(Ls); Ls += dpp<XOR2>(Ls); Ls += dpp<XOR7>(Ls);
            const float wn = we / Ls;
            LAS float* wl = (LAS float*)(L + ST1_OFF + 128) + hw * 32 + g * 8;
            if (c < 8) wl[c] = wn;
            CBAR();
            const f32x4 wa = *(const LAS f32x4*)wl, wb = *(const LAS f32x4*)(wl + 4);
            float v0 = 0.f, v1 = 0.f, v2 = 0.f, v3 = 0.f;
#pragma unroll
            for (int i = 0; i < 8; ++i) {
                const float wi = i < 4 ? wa[i & 3] : wb[i & 3];
                v0 = fmaf(wi, bf_lo(o9[i].x), v0); v1 = fmaf(wi, bf_hi(o9[i].x), v1);
                v2 = fmaf(wi, bf_lo(o9[i].z), v2); v3 = fmaf(wi, bf_hi(o9[i].z), v3);
            }
            *(LAS v2u*)(L + OA_OFF + (4 * hw + g) * AC_STR + (8 * (c & 7) + 4 * (c >> 3)) * 2) = v2u{pk_bf16(v0, v1), pk_bf16(v2, v3)};
            lds_inc(FL + FL_O, lane);
            STAMP(6);
        }
        if (hw == 1) {
            const int nsl = bid & 3, j = bid >> 2, st = j >> 3, G = (j & 7) >> 1, lam = lane & 7, cc = 8 * (j & 1) + lam, src = lane >> 3;
            const int b = 4 * (G & 1) + (st >> 1), n = 256 * nsl + 128 * (G >> 1) + 8 * cc + 4 * (st & 1);
            const v2u xx = *(const GAS v2u*)(p.x + b * D + n), bb = *(const GAS v2u*)(p.b2 + n);
            const int uo = (st * 64 + 8 * (j & 7) + lam) * 16;
            float sv[4], yv[4];
            {
                int vo[8];
#pragma unroll
                for (int i = 0; i < 8; ++i) vo[i] = SP_OFF + (nsl * 64 + 8 * i + src) * 8192 + uo;
                lds_wait_ge(FL + FL_ST, 1);
                __builtin_amdgcn_s_sleep(MK_DF);
                v4u f8[8];
                gather<8>(rs, vo, tag, f8);
                sv[0] = sv[1] = sv[2] = sv[3] = 0.f;
#pragma unroll
                for (int i = 0; i < 8; ++i) { sv[0] += bf_lo(f8[i].x); sv[1] += bf_hi(f8[i].x); sv[2] += bf_lo(f8[i].z); sv[3] += bf_hi(f8[i].z); }
#pragma unroll
                for (int q = 0; q < 4; ++q) { sv[q] += dpp<XOR8>(sv[q]); sv[q] = xrow16_sum(sv[q]); }
            }
            STAMP(7);
            {
                int vo[7];
#pragma unroll
                for (int i = 0; i < 7; ++i) vo[i] = YP_OFF + (nsl * 56 + 8 * i + src) * 8192 + uo;
                lds_wait_ge(FL + FL_YT, 1);
                __builtin_amdgcn_s_sleep(MK_DF);
                v4u f7[7];
                gather<7>(rs, vo, tag, f7);
                yv[0] = yv[1] = yv[2] = yv[3] = 0.f;
#pragma unroll
                for (int i = 0; i < 7; ++i) { yv[0] += bf_lo(f7[i].x); yv[1] += bf_hi(f7[i].x); yv[2] += bf_lo(f7[i].z); yv[3] += bf_hi(f7[i].z); }
#pragma unroll
                for (int q = 0; q < 4; ++q) { yv[q] += dpp<XOR8>(yv[q]); yv[q] = xrow16_sum(yv[q]); }
            }
            const float o0 = bf_lo(xx.x) + sv[0] * silu(yv[0] + bf_lo(bb.x)), o1 = bf_hi(xx.x) + sv[1] * silu(yv[1] + bf_hi(bb.x));
            const float o2 = bf_lo(xx.y) + sv[2] * silu(yv[2] + bf_lo(bb.y)), o3 = bf_hi(xx.y) + sv[3] * silu(yv[3] + bf_hi(bb.y));
            if (lane < 8) *(GAS v2u*)(p.out + b * D + n) = v2u{pk_bf16(o0, o1), pk_bf16(o2, o3)};
            if (bid == 0 && lane == 0) __hip_atomic_store((GAS unsigned*)(p.ws + WS_EPOCH), tag, __ATOMIC_RELAXED, __HIP_MEMORY_SCOPE_AGENT);
            STAMP(1);
        }
    }
}

}

extern "C" void kernel_launch(void* const* d_in, const int* in_sizes, int n_in, void* d_out, int out_size, void* d_ws, size_t ws_size,
                              hipStream_t stream) {
    mk::Params p;
    p.x = (const u16*)d_in[0];
    p.kc = (const u16*)d_in[1];
    p.vc = (const u16*)d_in[2];
    p.wq = (const u16*)d_in[3];
    p.wo = (const u16*)d_in[4];
    p.w1 = (const u16*)d_in[6];
    p.b1 = (const u16*)d_in[7];
    p.w2 = (const u16*)d_in[8];
    p.b2 = (const u16*)d_in[9];
    p.out = (u16*)d_out;
    p.ws = (unsigned char*)d_ws;
    mk::fused_kernel<<<256, 512, 0, stream>>>(p);
}
```

```cpp
#include <hip/hip_runtime.h>
#include <hip/hip_bf16.h>
#include <stdint.h>

#define GAS __attribute__((address_space(1)))
#define LAS __attribute__((address_space(3)))
typedef unsigned short u16;
typedef unsigned v4u __attribute__((ext_vector_type(4)));
typedef unsigned v2u __attribute__((ext_vector_type(2)));
typedef float f32x4 __attribute__((ext_vector_type(4)));
typedef float f32x2 __attribute__((ext_vector_type(2)));
typedef __bf16 bf16x8 __attribute__((ext_vector_type(8)));
typedef __bf16 bf16x2 __attribute__((ext_vector_type(2)));

#define MK_RTDELAY 5000
#define MK_STAMPS 0
#define MK_WCH 6
#define MK_ORDER 0
#define MK_CSLEEP 0
#define MK_DQ 16
#define MK_DO 16
#define MK_DH 16
#define MK_DF 8

namespace mk {

constexpr int B = 8, D = 1024, SK = 4096, DH = 128, WIN = 1024, F = 3584, NQ = 4096;
constexpr int NCH = 8, CH = 4, WCH = MK_WCH;

constexpr int WS_EPOCH = 0;
constexpr int QP_ROW = 16384, QP_OFF = 4096;
constexpr int HP_ROW = 14336, HP_OFF = QP_OFF + 32 * QP_ROW;
constexpr int OP_OFF = HP_OFF + 32 * HP_ROW;
constexpr int OM_OFF = OP_OFF + 32 * 8 * 8 * 512;
constexpr int SP_OFF = OM_OFF + 32 * 8 * 8 * 16;
constexpr int YP_OFF = SP_OFF + 4 * 64 * 8192;
constexpr int WS_END = YP_OFF + 4 * 56 * 8192;
constexpr int ST_OFF = WS_END;
constexpr int WS_BYTES = ST_OFF + 256 * 8 * 16 * 8;

constexpr int XN_STR = 2048 + 16, XN_OFF = 0;
constexpr int QS_STR = 256 + 16, QS_OFF = XN_OFF + 8 * XN_STR;
constexpr int AC_STR = 128 + 16, OA_OFF = QS_OFF + 8 * QS_STR;
constexpr int HA_OFF = OA_OFF + 8 * AC_STR;
constexpr int ZR_OFF = HA_OFF + 8 * AC_STR;
constexpr int KV_OFF = ZR_OFF + 128;
constexpr int ST1_OFF = KV_OFF + 4096;
constexpr int FL_OFF = ST1_OFF + 512;
constexpr int KS_OFF = FL_OFF + 256;
constexpr int LDS_USED = KS_OFF + 32768;
constexpr int LDS_BYTES = 96 * 1024;
static_assert(LDS_USED + 1024 <= LDS_BYTES && (KS_OFF % 16) == 0 && (KV_OFF % 16) == 0 && (ZR_OFF % 16) == 0, "lds");
enum { FL_PROG = 0  , FL_XN = 8, FL_Q = 9, FL_KV1 = 10, FL_O = 11, FL_H = 12, FL_T = 13, FL_TV = 14, FL_HT = 15, FL_OT = 16, FL_ST = 17, FL_QT = 18, FL_YT = 19 };

struct Params {
    const u16 *x, *kc, *vc, *wq, *wo, *w1, *b1, *w2, *b2;
    u16* out;
    unsigned char* ws;
};

constexpr unsigned kSpinCap = 1u << 16;
constexpr int AUX_NT = 2, AUX_SC1 = 16;

__device__ __forceinline__ float bf_lo(unsigned u) { return __builtin_bit_cast(float, u << 16); }
__device__ __forceinline__ float bf_hi(unsigned u) { return __builtin_bit_cast(float, u & 0xffff0000u); }
__device__ __forceinline__ unsigned pk_bf16(float lo, float hi) {
    f32x2 v = {lo, hi};
    return __builtin_bit_cast(unsigned, __builtin_convertvector(v, bf16x2));
}
__device__ __forceinline__ f32x4 mfma(v4u a, v4u b, f32x4 c) {
    return __builtin_amdgcn_mfma_f32_16x16x32_bf16(__builtin_bit_cast(bf16x8, a), __builtin_bit_cast(bf16x8, b), c, 0, 0, 0);
}
__device__ __forceinline__ unsigned f2u(float f) { return __builtin_bit_cast(unsigned, f); }
__device__ __forceinline__ float u2f(unsigned u) { return __builtin_bit_cast(float, u); }

#define CBAR() asm volatile("" ::: "memory")
template <int N> __device__ __forceinline__ void waitvm() { asm volatile("s_waitcnt vmcnt(%0)" ::"i"(N) : "memory"); }

__device__ __forceinline__ void lds_set(LAS unsigned* p, unsigned v) {
    CBAR();
    __hip_atomic_store(p, v, __ATOMIC_RELAXED, __HIP_MEMORY_SCOPE_WORKGROUP);
    CBAR();
}
__device__ __forceinline__ void lds_inc(LAS unsigned* p, int lane) {
    CBAR();
    if (lane == 0) __hip_atomic_fetch_add(p, 1u, __ATOMIC_RELAXED, __HIP_MEMORY_SCOPE_WORKGROUP);
    CBAR();
}
__device__ __forceinline__ unsigned lds_get(LAS unsigned* p) {
    return (unsigned)__builtin_amdgcn_readfirstlane(__hip_atomic_load(p, __ATOMIC_RELAXED, __HIP_MEMORY_SCOPE_WORKGROUP));
}
__device__ __forceinline__ void lds_wait_ge(LAS unsigned* p, unsigned v) {
#pragma nounroll
    for (unsigned spins = 0; spins < kSpinCap; ++spins) {
        if (lds_get(p) >= v) break;
        __builtin_amdgcn_s_sleep(1);
    }
    CBAR();
}

__device__ __forceinline__ float xrow16_max(float x) {
    auto s = __builtin_amdgcn_permlane16_swap(f2u(x), f2u(x), false, false);
    x = fmaxf(u2f(s[0]), u2f(s[1]));
    auto t = __builtin_amdgcn_permlane32_swap(f2u(x), f2u(x), false, false);
    return fmaxf(u2f(t[0]), u2f(t[1]));
}
__device__ __forceinline__ float xrow16_sum(float x) {
    auto s = __builtin_amdgcn_permlane16_swap(f2u(x), f2u(x), false, false);
    x = u2f(s[0]) + u2f(s[1]);
    auto t = __builtin_amdgcn_permlane32_swap(f2u(x), f2u(x), false, false);
    return u2f(t[0]) + u2f(t[1]);
}
template <int CTRL> __device__ __forceinline__ float dpp(float x) {
    return u2f(__builtin_amdgcn_mov_dpp(f2u(x), CTRL, 0xf, 0xf, true));
}
constexpr int XOR1 = 0xB1, XOR2 = 0x4E, XOR7 = 0x141, XOR8 = 0x128;
__device__ __forceinline__ float row16_sum(float x) {
    x += dpp<XOR1>(x); x += dpp<XOR2>(x); x += dpp<XOR7>(x); x += dpp<XOR8>(x);
    return x;
}
__device__ __forceinline__ float wave_sum(float x) { return xrow16_sum(row16_sum(x)); }

#define STAMP(i) do {} while (0)

__device__ __forceinline__ void transpose8(v4u* r) {
    v4u o[8];
#pragma unroll
    for (int t = 0; t < 4; ++t)
#pragma unroll
        for (int jp = 0; jp < 4; ++jp) {
            const unsigned x0 = r[2 * jp][t], x1 = r[2 * jp + 1][t];
            o[2 * t][jp] = __builtin_amdgcn_perm(x1, x0, 0x05040100u);
            o[2 * t + 1][jp] = __builtin_amdgcn_perm(x1, x0, 0x07060302u);
        }
#pragma unroll
    for (int e = 0; e < 8; ++e) { asm volatile("" : "+v"(o[e])); r[e] = o[e]; }
}

__device__ __forceinline__ void lds_wait_ge_fast(LAS unsigned* p, unsigned v) {
#pragma nounroll
    for (unsigned spins = 0; spins < 16u * kSpinCap; ++spins) {
        if (lds_get(p) >= v) break;
    }
    CBAR();
}
template <int K, class LD>
__device__ __forceinline__ void stream_head(LD& ld, v4u (&r)[32], LAS unsigned* prev) {
    if constexpr (K == 0) lds_wait_ge(prev, NCH + K - WCH > 0 ? NCH + K - WCH : 1);
    lds_wait_ge_fast(prev, NCH + K - WCH + 1);
#pragma unroll
    for (int i = 0; i < CH; ++i) r[K * CH + i] = ld(K * CH + i);
    __builtin_amdgcn_sched_barrier(0);
    if constexpr (K + 1 < WCH) stream_head<K + 1>(ld, r, prev);
}
template <int K, class LD, class ON>
__device__ __forceinline__ void stream_step(LD& ld, ON& on, v4u (&r)[32], LAS unsigned* me) {
    if constexpr (K < NCH) {
        waitvm<(WCH - 1) * CH>();
        lds_set(me, K - WCH + 1);
#pragma unroll
        for (int i = 0; i < CH; ++i) r[K * CH + i] = ld(K * CH + i);
        __builtin_amdgcn_sched_barrier(0);
        on.template run<K - WCH>(r);
        __builtin_amdgcn_sched_barrier(0);
    } else {
        waitvm<(NCH + WCH - 1 - K) * CH>();
        lds_set(me, K - WCH + 1);
        on.template run<K - WCH>(r);
        __builtin_amdgcn_sched_barrier(0);
    }
    if constexpr (K + 1 < NCH + WCH) stream_step<K + 1>(ld, on, r, me);
}

template <int N>
__device__ __forceinline__ void gather(__amdgpu_buffer_rsrc_t rs, const int (&voff)[N], unsigned tag, v4u (&g)[N]) {
    unsigned pend = (1u << N) - 1u;
#pragma nounroll
    for (unsigned spins = 0; spins < kSpinCap; ++spins) {
        CBAR();
#pragma unroll
        for (int i = 0; i < N; ++i)
            if (pend & (1u << i)) g[i] = __builtin_amdgcn_raw_buffer_load_b128(rs, voff[i], 0, AUX_SC1);
#pragma unroll
        for (int i = 0; i < N; ++i)
            if ((pend & (1u << i)) && __all(g[i].y == tag && g[i].w == tag)) pend &= ~(1u << i);
        if (pend == 0u) break;
    }
    CBAR();
}

struct GemmOn {
    LAS unsigned* flag; unsigned need;
    const LAS unsigned char* pa0; const LAS unsigned char* pa1; int step;
    f32x4 (&acc)[8];
    v4u (&a)[4];
    int done;
    __device__ __forceinline__ void load_a() {
        a[0] = *(const LAS v4u*)(pa0); a[1] = *(const LAS v4u*)(pa1);
        a[2] = *(const LAS v4u*)(pa0 + step); a[3] = *(const LAS v4u*)(pa1 + step);
    }
    template <int G> __device__ __forceinline__ void mul(v4u (&r)[32]) {
#pragma unroll
        for (int e = 0; e < 8; ++e) acc[e] = mfma(a[G], r[8 * G + e], acc[e]);
    }
    template <int C> __device__ __forceinline__ void run(v4u (&r)[32]) {
        if constexpr (C & 1) {
            constexpr int G = C >> 1;
            transpose8(&r[8 * G]);
            if (done < 0 && lds_get(flag) >= need) {
                CBAR();
                load_a();
                done = 0;
                if constexpr (G >= 1) mul<0>(r);
                if constexpr (G >= 2) mul<1>(r);
                if constexpr (G >= 3) mul<2>(r);
            }
            if (done >= 0) mul<G>(r);
        }
    }
    __device__ __forceinline__ void finish(v4u (&r)[32]) {
        if (done < 0) {
            lds_wait_ge(flag, need);
            load_a();
            mul<0>(r); mul<1>(r); mul<2>(r); mul<3>(r);
        }
    }
};
struct KvOn {
    LAS unsigned char* ks; int lane;
    template <int C> __device__ __forceinline__ void run(v4u (&r)[32]) {
        if constexpr (C == 1 || C == 3) {
            constexpr int H = C >> 1;
            const int g = lane >> 4, c = lane & 15;
#pragma unroll
            for (int i = 0; i < 8; ++i) {
                const int key = 32 * H + 4 * i + g;
                *(LAS v4u*)(ks + key * 256 + ((c ^ ((4 * i + g) & 15)) * 16)) = r[8 * H + i];
            }
            CBAR();
#pragma unroll
            for (int t = 0; t < 2; ++t)
#pragma unroll
                for (int s = 0; s < 4; ++s) r[8 * H + 4 * t + s] = *(const LAS v4u*)(ks + (32 * H + 16 * t + c) * 256 + (((4 * s + g) ^ c) * 16));
        }
        if constexpr (C == 5) transpose8(&r[16]);
        if constexpr (C == 7) transpose8(&r[24]);
    }
};

__device__ __forceinline__ void fold_halves(const f32x4 (&acc)[8], int lane, float (&res)[8]) {
    const bool lo = (lane & 8) == 0;
#pragma unroll
    for (int e = 0; e < 8; ++e) {
        auto s = __builtin_amdgcn_permlane32_swap(f2u(acc[e][0]), f2u(acc[e][1]), false, false);
        auto t = __builtin_amdgcn_permlane32_swap(f2u(acc[e][2]), f2u(acc[e][3]), false, false);
        float s1 = u2f(s[0]) + dpp<XOR8>(u2f(s[1]));
        float s2 = u2f(t[1]) + dpp<XOR8>(u2f(t[0]));
        asm volatile("" : "+v"(s1), "+v"(s2));
        res[e] = lo ? s1 : s2;
    }
}

__device__ __forceinline__ void ln_rows(const v4u (&xv)[4], LAS unsigned char* dst, int lane) {
    float v[2][16], mean[2], rstd[2];
#pragma unroll
    for (int k = 0; k < 2; ++k) {
#pragma unroll
        for (int q = 0; q < 4; ++q) {
            v[k][2 * q] = bf_lo(xv[2 * k][q]); v[k][2 * q + 1] = bf_hi(xv[2 * k][q]);
            v[k][8 + 2 * q] = bf_lo(xv[2 * k + 1][q]); v[k][8 + 2 * q + 1] = bf_hi(xv[2 * k + 1][q]);
        }
        float s4[4];
#pragma unroll
        for (int q = 0; q < 4; ++q) s4[q] = (v[k][4 * q] + v[k][4 * q + 1]) + (v[k][4 * q + 2] + v[k][4 * q + 3]);
        mean[k] = (s4[0] + s4[1]) + (s4[2] + s4[3]);
    }
    mean[0] = wave_sum(mean[0]) * (1.0f / D);
    mean[1] = wave_sum(mean[1]) * (1.0f / D);
#pragma unroll
    for (int k = 0; k < 2; ++k) {
#pragma unroll
        for (int i = 0; i < 16; ++i) v[k][i] -= mean[k];
        float s4[4];
#pragma unroll
        for (int q = 0; q < 4; ++q)
            s4[q] = fmaf(v[k][4 * q], v[k][4 * q], v[k][4 * q + 1] * v[k][4 * q + 1]) + fmaf(v[k][4 * q + 2], v[k][4 * q + 2], v[k][4 * q + 3] * v[k][4 * q + 3]);
        rstd[k] = (s4[0] + s4[1]) + (s4[2] + s4[3]);
    }
    rstd[0] = wave_sum(rstd[0]);
    rstd[1] = wave_sum(rstd[1]);
#pragma unroll
    for (int k = 0; k < 2; ++k) {
        const float rs_ = 1.0f / sqrtf(rstd[k] * (1.0f / D) + 1e-5f);
#pragma unroll
        for (int h = 0; h < 2; ++h) {
            const v4u o = {pk_bf16(v[k][h * 8] * rs_, v[k][h * 8 + 1] * rs_), pk_bf16(v[k][h * 8 + 2] * rs_, v[k][h * 8 + 3] * rs_),
                           pk_bf16(v[k][h * 8 + 4] * rs_, v[k][h * 8 + 5] * rs_), pk_bf16(v[k][h * 8 + 6] * rs_, v[k][h * 8 + 7] * rs_)};
            *(LAS v4u*)(dst + k * XN_STR + h * 1024 + lane * 16) = o;
        }
    }
}
__device__ __forceinline__ void load_x_rows(const u16* x, int row0, int lane, v4u (&xv)[4]) {
    const GAS unsigned char* xp = (const GAS unsigned char*)x + row0 * 2048 + lane * 16;
#pragma unroll
    for (int i = 0; i < 4; ++i) xv[i] = *(const GAS v4u*)(xp + i * 1024);
}

__device__ __forceinline__ float gelu_tanh(float s) {
    const float u = 0.7978845608028654f * (s + 0.044715f * s * s * s);
    const float e = __expf(2.0f * u);
    const float th = 1.0f - 2.0f / (e + 1.0f);
    return 0.5f * s * (1.0f + th);
}
__device__ __forceinline__ float silu(float y) { return y / (1.0f + __expf(-y)); }

__global__ void __launch_bounds__(512, 2) fused_kernel(Params p) {
    __shared__ __attribute__((aligned(16))) unsigned char smem[LDS_BYTES];
    LAS unsigned char* L = (LAS unsigned char*)smem;
    LAS unsigned* FL = (LAS unsigned*)(L + FL_OFF);
    const int tid = threadIdx.x, lane = tid & 63, w = __builtin_amdgcn_readfirstlane(tid >> 6), bid = blockIdx.x;
    const int g = lane >> 4, c = lane & 15;
    const __amdgpu_buffer_rsrc_t rs = __builtin_amdgcn_make_buffer_rsrc((void*)p.ws, 0, WS_BYTES, 0x00020000);
    STAMP(0);
    constexpr float C2 = 0.08838834764831845f * 1.4426950408889634f;

    const int xcd = bid & 7, idx = bid >> 3;
    const bool has_w1 = bid < 224;
    const bool has_w2 = idx < 28;
    const int ks_o = xcd * 8 + (idx >> 2), nsl_o = idx & 3;
    const int t2 = xcd * 28 + idx, ks_2 = t2 >> 2, nsl_2 = t2 & 3;
    int prev;
    if (MK_ORDER == 0) prev = (w == 4 && !has_w1) ? 2 : w - 1;
    else prev = w == 3 ? 0 : w == 1 ? (has_w1 ? 3 : 0) : w == 5 ? 2 : w == 4 ? (has_w2 ? 5 : 2) : w - 1;

    const bool is_gemm = (w == 0 || w == 3 || w == 4 || w == 5);
    if (is_gemm) {
        v4u r[32];
        const bool ta = (w == 0 || w == 3);
        const u16* W = w == 0 ? p.wq : w == 3 ? p.w1 : w == 4 ? p.wo : p.w2;
        const int ldw = w == 0 ? NQ * 2 : w == 3 ? F * 2 : D * 2;
        const int nbytes = w == 3 || w == 5 ? D * F * 2 : D * NQ * 2;
        const int ks = ta ? (bid & 3) : w == 4 ? ks_o : ks_2;
        const int ns = ta ? (bid >> 2) : w == 4 ? nsl_o : nsl_2;
        const __amdgpu_buffer_rsrc_t rw = __builtin_amdgcn_make_buffer_rsrc((void*)W, 0, nbytes, 0x00020000);
        const int voffw = ta ? (ks * 256 + 32 * (c >> 3) + 8 * g) * ldw + (ns * 64 + 8 * (c & 7)) * 2
                             : (ks * 64 + 8 * g) * ldw + (ns * 256 + 8 * c) * 2;
        const int go1 = ta ? 64 * ldw : 256, go2 = ta ? 128 * ldw : 32 * ldw;
        auto ldg = [&](int i) {
            return __builtin_amdgcn_raw_buffer_load_b128(rw, voffw, (i & 7) * ldw + ((i >> 3) & 1) * go1 + (i >> 4) * go2, AUX_NT);
        };
        if (w == 0) {
            FL[lane] = 0u;
            if (lane < 32) ((LAS unsigned*)(L + ZR_OFF))[lane] = 0u;
#pragma unroll
            for (int i = 0; i < WCH * CH; ++i) r[i] = ldg(i);
            __builtin_amdgcn_sched_barrier(0);
            __syncthreads();
        } else {
            v4u xv[4];
            if (w >= 4) load_x_rows(p.x, (w - 4) * 2, lane, xv);
            __syncthreads();
            if (w >= 4) { ln_rows(xv, L + XN_OFF + (w - 4) * 2 * XN_STR, lane); lds_inc(FL + FL_XN, lane); STAMP(8); }
        }
        const bool has = w == 3 ? has_w1 : w == 5 ? has_w2 : true;
        LAS unsigned* trig = FL + (w == 0 ? FL_QT : w == 3 ? FL_HT : w == 4 ? FL_ST : FL_YT);
        if (has) {
            unsigned bbias = 0;
            if (w != 0) stream_head<0>(ldg, r, FL + FL_PROG + prev);
            f32x4 acc[8];
#pragma unroll
            for (int e = 0; e < 8; ++e) acc[e] = f32x4{0.f, 0.f, 0.f, 0.f};
            v4u a[4];
            const LAS unsigned char* pa0; const LAS unsigned char* pa1; int step;
            if (ta) {
                pa0 = L + XN_OFF + ks * 512 + (c & 7) * XN_STR + (32 * (c >> 3) + 8 * g) * 2; pa1 = pa0 + 128; step = 256;
            } else {
                const LAS unsigned char* act = L + (w == 4 ? OA_OFF : HA_OFF) + (c & 7) * AC_STR + g * 16;
                const LAS unsigned char* zr = L + ZR_OFF + g * 16;
                pa0 = c < 8 ? act : zr; pa1 = c < 8 ? zr : act; step = 64;
            }
            GemmOn on{FL + (ta ? FL_XN : w == 4 ? FL_O : FL_H), ta ? 4u : w == 4 ? 2u : 1u, pa0, pa1, step, acc, a, -1};
            stream_step<WCH>(ldg, on, r, FL + FL_PROG + w);
            STAMP(2);
            on.finish(r);
            STAMP(3);
            lds_wait_ge(FL + FL_T, 1);
            const unsigned tag = __builtin_amdgcn_readfirstlane(FL[FL_TV]);
            __builtin_amdgcn_s_setprio(3);
            if (ta) {
                float res[8];
                fold_halves(acc, lane, res);
                const int b = 4 * (g & 1) + (g >> 1) + 2 * (c >> 3);
                const int o = (w == 0 ? QP_OFF + (ks * 8 + b) * QP_ROW : HP_OFF + (ks * 8 + b) * HP_ROW) + ns * 256 + (c & 7) * 16;
#pragma unroll
                for (int u = 0; u < 2; ++u)
                    __builtin_amdgcn_raw_buffer_store_b128(v4u{pk_bf16(res[4 * u], res[4 * u + 1]), tag, pk_bf16(res[4 * u + 2], res[4 * u + 3]), tag}, rs, o, u * 128, AUX_SC1);
            } else {
                const int o = (w == 4 ? SP_OFF + (ns * 64 + ks) * 8192 : YP_OFF + (ns * 56 + ks) * 8192) + lane * 16;
#pragma unroll
                for (int rr = 0; rr < 4; ++rr)
#pragma unroll
                    for (int eh = 0; eh < 2; ++eh)
                        __builtin_amdgcn_raw_buffer_store_b128(
                            v4u{pk_bf16(acc[4 * eh][rr], acc[4 * eh + 1][rr]), tag, pk_bf16(acc[4 * eh + 2][rr], acc[4 * eh + 3][rr]), tag}, rs, o,
                            (2 * rr + eh) * 1024, AUX_SC1);
            }
            lds_set(trig, 1);
            STAMP(4);
            (void)bbias;
        } else {
            if (w == 3) lds_wait_ge(FL + FL_PROG + (MK_ORDER == 0 ? 4 : 1), NCH / 2);
            else lds_wait_ge(FL + FL_PROG + 4, NCH);
            lds_set(trig, 1);
        }
        if (w == 3 && has_w2) {
            lds_wait_ge(FL + FL_T, 1);
            const unsigned tag = __builtin_amdgcn_readfirstlane(FL[FL_TV]);
            const int n4 = 8 * (c & 7) + 4 * (c >> 3);
            const v2u bb = *(const GAS v2u*)(p.b1 + ks_2 * 64 + n4);
            int vo[8];
#pragma unroll
            for (int i = 0; i < 8; ++i) vo[i] = HP_OFF + ((i >> 1) * 8 + (i & 1) * 4 + g) * HP_ROW + ks_2 * 256 + c * 16;
            __builtin_amdgcn_s_sleep(MK_DH);
            v4u h8[8];
            gather<8>(rs, vo, tag, h8);
            STAMP(5);
#pragma unroll
            for (int bh = 0; bh < 2; ++bh) {
                float v0 = (bf_lo(h8[bh].x) + bf_lo(h8[2 + bh].x)) + (bf_lo(h8[4 + bh].x) + bf_lo(h8[6 + bh].x)) + bf_lo(bb.x);
                float v1 = (bf_hi(h8[bh].x) + bf_hi(h8[2 + bh].x)) + (bf_hi(h8[4 + bh].x) + bf_hi(h8[6 + bh].x)) + bf_hi(bb.x);
                float v2 = (bf_lo(h8[bh].z) + bf_lo(h8[2 + bh].z)) + (bf_lo(h8[4 + bh].z) + bf_lo(h8[6 + bh].z)) + bf_lo(bb.y);
                float v3 = (bf_hi(h8[bh].z) + bf_hi(h8[2 + bh].z)) + (bf_hi(h8[4 + bh].z) + bf_hi(h8[6 + bh].z)) + bf_hi(bb.y);
                *(LAS v2u*)(L + HA_OFF + (bh * 4 + g) * AC_STR + n4 * 2) = v2u{pk_bf16(gelu_tanh(v0), gelu_tanh(v1)), pk_bf16(gelu_tanh(v2), gelu_tanh(v3))};
            }
            lds_set(FL + FL_H, 1);
            STAMP(6);
        }
    } else if (w == 1 || w == 2) {
        const int gid = xcd * 4 + (idx >> 3), sp = idx & 7;
        const size_t kvbase = ((size_t)gid * SK + (SK - WIN) + sp * 128 + (w - 1) * 64) * (DH * 2);
        const __amdgpu_buffer_rsrc_t rk = __builtin_amdgcn_make_buffer_rsrc((void*)((const unsigned char*)p.kc + kvbase), 0, 64 * 256, 0x00020000);
        const __amdgpu_buffer_rsrc_t rv = __builtin_amdgcn_make_buffer_rsrc((void*)((const unsigned char*)p.vc + kvbase), 0, 64 * 256, 0x00020000);
        const int vvo = g * 1024 + c * 16;
        auto ld = [&](int i) {
            if (i < 16) return __builtin_amdgcn_raw_buffer_load_b128(rk, lane * 16, i * 1024, AUX_NT);
            const int j = i - 16;
            return __builtin_amdgcn_raw_buffer_load_b128(rv, vvo, (j >> 3) * 8192 + ((j >> 2) & 1) * 4096 + (j & 3) * 256, AUX_NT);
        };
        KvOn on{L + KS_OFF + (w - 1) * 16384, lane};
        v4u r[32];
        __syncthreads();
        stream_head<0>(ld, r, FL + FL_PROG + prev);
        stream_step<WCH>(ld, on, r, FL + FL_PROG + w);
        STAMP(2);
        lds_wait_ge(FL + FL_Q, 2);
        STAMP(3);
        __builtin_amdgcn_s_setprio(3);
        v4u qf[4];
#pragma unroll
        for (int s = 0; s < 4; ++s) qf[s] = *(const LAS v4u*)(L + QS_OFF + (c & 7) * QS_STR + (32 * s + 8 * g) * 2);
        f32x4 S[4];
#pragma unroll
        for (int t = 0; t < 4; ++t) S[t] = f32x4{0.f, 0.f, 0.f, 0.f};
#pragma unroll
        for (int s = 0; s < 4; ++s)
#pragma unroll
            for (int t = 0; t < 4; ++t) S[t] = mfma(r[4 * t + s], qf[s], S[t]);
        float mx = fmaxf(fmaxf(fmaxf(S[0][0], S[0][1]), fmaxf(S[0][2], S[0][3])), fmaxf(fmaxf(S[1][0], S[1][1]), fmaxf(S[1][2], S[1][3])));
        mx = fmaxf(mx, fmaxf(fmaxf(fmaxf(S[2][0], S[2][1]), fmaxf(S[2][2], S[2][3])), fmaxf(fmaxf(S[3][0], S[3][1]), fmaxf(S[3][2], S[3][3]))));
        mx = xrow16_max(mx);
        LAS float* st1 = (LAS float*)(L + ST1_OFF);
        f32x4 O[4];
        float lsum = 0.f, m = mx;
        if (w == 2) {
            lds_wait_ge(FL + FL_KV1, 1);
            const float m1 = st1[c & 7], l1 = st1[8 + (c & 7)];
            m = fmaxf(mx, m1);
            const float f1 = __builtin_amdgcn_exp2f((m1 - m) * C2);
            lsum = (g == 0) ? f1 * l1 : 0.f;
            if (lane < 8) st1[16 + lane] = f1;
            CBAR();
            const f32x4 f4 = *(const LAS f32x4*)(st1 + 16 + 4 * (g & 1));
#pragma unroll
            for (int e = 0; e < 4; ++e) O[e] = *(const LAS f32x4*)(L + KV_OFF + (e * 64 + lane) * 16) * f4;
        } else {
#pragma unroll
            for (int e = 0; e < 4; ++e) O[e] = f32x4{0.f, 0.f, 0.f, 0.f};
        }
        const float mc = m * C2;
        float ls4[4];
#pragma unroll
        for (int t = 0; t < 4; ++t) {
#pragma unroll
            for (int q = 0; q < 4; ++q) S[t][q] = __builtin_amdgcn_exp2f(fmaf(S[t][q], C2, -mc));
            ls4[t] = (S[t][0] + S[t][1]) + (S[t][2] + S[t][3]);
        }
        lsum += (ls4[0] + ls4[1]) + (ls4[2] + ls4[3]);
        const bool lo8 = c < 8;
#pragma unroll
        for (int u = 0; u < 2; ++u) {
            const v4u pa = {pk_bf16(S[2 * u][0], S[2 * u][1]), pk_bf16(S[2 * u][2], S[2 * u][3]),
                            pk_bf16(S[2 * u + 1][0], S[2 * u + 1][1]), pk_bf16(S[2 * u + 1][2], S[2 * u + 1][3])};
            const v4u z = {0u, 0u, 0u, 0u};
            const v4u p0 = lo8 ? pa : z, p1 = lo8 ? z : pa;
#pragma unroll
            for (int e = 0; e < 4; ++e) O[e] = mfma(p0, r[16 + 8 * u + e], O[e]);
#pragma unroll
            for (int e = 0; e < 4; ++e) O[e] = mfma(p1, r[16 + 8 * u + 4 + e], O[e]);
        }
        lsum = xrow16_sum(lsum);
        if (w == 1) {
#pragma unroll
            for (int e = 0; e < 4; ++e) *(LAS f32x4*)(L + KV_OFF + (e * 64 + lane) * 16) = O[e];
            if (lane < 8) { st1[lane] = m; st1[8 + lane] = lsum; }
            lds_set(FL + FL_KV1, 1);
            STAMP(4);
        } else {
            const unsigned tag = __builtin_amdgcn_readfirstlane(FL[FL_TV]);
            const int obase = OP_OFF + ((gid * 8 + sp) * 8 + 4 * (g & 1)) * 512 + (g >> 1) * 256 + c * 16;
#pragma unroll
            for (int rr = 0; rr < 4; ++rr)
                __builtin_amdgcn_raw_buffer_store_b128(v4u{pk_bf16(O[0][rr], O[1][rr]), tag, pk_bf16(O[2][rr], O[3][rr]), tag}, rs, obase, rr * 512, AUX_SC1);
            if (lane < 8) __builtin_amdgcn_raw_buffer_store_b128(v4u{f2u(m), tag, f2u(lsum), tag}, rs, OM_OFF + ((gid * 8 + lane) * 8 + sp) * 16, 0, AUX_SC1);
            lds_set(FL + FL_OT, 1);
            STAMP(4);
        }
    } else {
        const int hw = w - 6;
        unsigned ep = 0;
        if (hw == 1) ep = __hip_atomic_load((GAS unsigned*)(p.ws + WS_EPOCH), __ATOMIC_RELAXED, __HIP_MEMORY_SCOPE_AGENT);
        unsigned touch = 0;
        {
            v4u xv[4];
            load_x_rows(p.x, 4 + hw * 2, lane, xv);
            __syncthreads();
            if (hw == 1) {
                FL[FL_TV] = __builtin_amdgcn_readfirstlane(ep) + 1u;
                lds_set(FL + FL_T, 1);
            }
            ln_rows(xv, L + XN_OFF + (4 + hw * 2) * XN_STR, lane);
            lds_inc(FL + FL_XN, lane);
            STAMP(8);
            asm volatile("" :: "v"(touch));
        }
        __builtin_amdgcn_s_setprio(3);
        lds_wait_ge(FL + FL_T, 1);
        const unsigned tag = __builtin_amdgcn_readfirstlane(FL[FL_TV]);
        {
            const int gid = xcd * 4 + (idx >> 3), bq = gid >> 2, hk = gid & 3;
            int vo[8];
#pragma unroll
            for (int i = 0; i < 8; ++i) vo[i] = QP_OFF + ((i >> 1) * 8 + bq) * QP_ROW + (hk * 16 + hw * 8 + (i & 1) * 4 + g) * 256 + c * 16;
            lds_wait_ge(FL + FL_QT, 1);
            __builtin_amdgcn_s_sleep(MK_DQ);
            v4u q8[8];
            gather<8>(rs, vo, tag, q8);
            STAMP(3);
            const int n4 = 8 * (c & 7) + 4 * (c >> 3);
#pragma unroll
            for (int sh = 0; sh < 2; ++sh) {
                const float v0 = (bf_lo(q8[sh].x) + bf_lo(q8[2 + sh].x)) + (bf_lo(q8[4 + sh].x) + bf_lo(q8[6 + sh].x));
                const float v1 = (bf_hi(q8[sh].x) + bf_hi(q8[2 + sh].x)) + (bf_hi(q8[4 + sh].x) + bf_hi(q8[6 + sh].x));
                const float v2 = (bf_lo(q8[sh].z) + bf_lo(q8[2 + sh].z)) + (bf_lo(q8[4 + sh].z) + bf_lo(q8[6 + sh].z));
                const float v3 = (bf_hi(q8[sh].z) + bf_hi(q8[2 + sh].z)) + (bf_hi(q8[4 + sh].z) + bf_hi(q8[6 + sh].z));
                const int n = (hw * 8 + sh * 4 + g) * 64 + n4;
                *(LAS v2u*)(L + QS_OFF + (n >> 7) * QS_STR + (n & 127) * 2) = v2u{pk_bf16(v0, v1), pk_bf16(v2, v3)};
            }
            lds_inc(FL + FL_Q, lane);
            STAMP(4);
        }
        {
            const int head = ks_o >> 1, dh = ks_o & 1, hk = head >> 3, hh = head & 7;
            const int gidb = (4 * hw + g) * 4 + hk;
            int vo[9];
#pragma unroll
            for (int i = 0; i < 8; ++i) vo[i] = OP_OFF + ((gidb * 8 + i) * 8 + hh) * 512 + (c >> 3) * 256 + (8 * dh + (c & 7)) * 16;
            vo[8] = OM_OFF + ((gidb * 8 + hh) * 8 + (c & 7)) * 16;
            lds_wait_ge(FL + FL_OT, 1);
            __builtin_amdgcn_s_sleep(MK_DO);
            v4u o9[9];
            gather<9>(rs, vo, tag, o9);
            STAMP(5);
            const float ms = u2f(o9[8].x), ls = u2f(o9[8].z);
            float M = ms;
            M = fmaxf(M, dpp<XOR1>(M)); M = fmaxf(M, dpp<XOR2>(M)); M = fmaxf(M, dpp<XOR7>(M));
            const float we = __builtin_amdgcn_exp2f((ms - M) * C2);
            float Ls = we * ls;
            Ls += dpp<XOR1>(Ls); Ls += dpp<XOR2>(Ls); Ls += dpp<XOR7>(Ls);
            const float wn = we / Ls;
            LAS float* wl = (LAS float*)(L + ST1_OFF + 128) + hw * 32 + g * 8;
            if (c < 8) wl[c] = wn;
            CBAR();
            const f32x4 wa = *(const LAS f32x4*)wl, wb = *(const LAS f32x4*)(wl + 4);
            float v0 = 0.f, v1 = 0.f, v2 = 0.f, v3 = 0.f;
#pragma unroll
            for (int i = 0; i < 8; ++i) {
                const float wi = i < 4 ? wa[i & 3] : wb[i & 3];
                v0 = fmaf(wi, bf_lo(o9[i].x), v0); v1 = fmaf(wi, bf_hi(o9[i].x), v1);
                v2 = fmaf(wi, bf_lo(o9[i].z), v2); v3 = fmaf(wi, bf_hi(o9[i].z), v3);
            }
            *(LAS v2u*)(L + OA_OFF + (4 * hw + g) * AC_STR + (8 * (c & 7) + 4 * (c >> 3)) * 2) = v2u{pk_bf16(v0, v1), pk_bf16(v2, v3)};
            lds_inc(FL + FL_O, lane);
            STAMP(6);
        }
        if (hw == 1) {
            const int nsl = bid & 3, j = bid >> 2, st = j >> 3, G = (j & 7) >> 1, lam = lane & 7, cc = 8 * (j & 1) + lam, src = lane >> 3;
            const int b = 4 * (G & 1) + (st >> 1), n = 256 * nsl + 128 * (G >> 1) + 8 * cc + 4 * (st & 1);
            const v2u xx = *(const GAS v2u*)(p.x + b * D + n), bb = *(const GAS v2u*)(p.b2 + n);
            const int uo = (st * 64 + 8 * (j & 7) + lam) * 16;
            float sv[4], yv[4];
            {
                int vo[8];
#pragma unroll
                for (int i = 0; i < 8; ++i) vo[i] = SP_OFF + (nsl * 64 + 8 * i + src) * 8192 + uo;
                lds_wait_ge(FL + FL_ST, 1);
                __builtin_amdgcn_s_sleep(MK_DF);
                v4u f8[8];
                gather<8>(rs, vo, tag, f8);
                sv[0] = sv[1] = sv[2] = sv[3] = 0.f;
#pragma unroll
                for (int i = 0; i < 8; ++i) { sv[0] += bf_lo(f8[i].x); sv[1] += bf_hi(f8[i].x); sv[2] += bf_lo(f8[i].z); sv[3] += bf_hi(f8[i].z); }
#pragma unroll
                for (int q = 0; q < 4; ++q) { sv[q] += dpp<XOR8>(sv[q]); sv[q] = xrow16_sum(sv[q]); }
            }
            STAMP(7);
            {
                int vo[7];
#pragma unroll
                for (int i = 0; i < 7; ++i) vo[i] = YP_OFF + (nsl * 56 + 8 * i + src) * 8192 + uo;
                lds_wait_ge(FL + FL_YT, 1);
                __builtin_amdgcn_s_sleep(MK_DF);
                v4u f7[7];
                gather<7>(rs, vo, tag, f7);
                yv[0] = yv[1] = yv[2] = yv[3] = 0.f;
#pragma unroll
                for (int i = 0; i < 7; ++i) { yv[0] += bf_lo(f7[i].x); yv[1] += bf_hi(f7[i].x); yv[2] += bf_lo(f7[i].z); yv[3] += bf_hi(f7[i].z); }
#pragma unroll
                for (int q = 0; q < 4; ++q) { yv[q] += dpp<XOR8>(yv[q]); yv[q] = xrow16_sum(yv[q]); }
            }
            const float o0 = bf_lo(xx.x) + sv[0] * silu(yv[0] + bf_lo(bb.x)), o1 = bf_hi(xx.x) + sv[1] * silu(yv[1] + bf_hi(bb.x));
            const float o2 = bf_lo(xx.y) + sv[2] * silu(yv[2] + bf_lo(bb.y)), o3 = bf_hi(xx.y) + sv[3] * silu(yv[3] + bf_hi(bb.y));
            if (lane < 8) *(GAS v2u*)(p.out + b * D + n) = v2u{pk_bf16(o0, o1), pk_bf16(o2, o3)};
            if (bid == 0 && lane == 0) __hip_atomic_store((GAS unsigned*)(p.ws + WS_EPOCH), tag, __ATOMIC_RELAXED, __HIP_MEMORY_SCOPE_AGENT);
            STAMP(1);
        }
    }
    if (w == 7) { const unsigned long long t0_ = __builtin_amdgcn_s_memrealtime(); while (__builtin_amdgcn_s_memrealtime() - t0_ < MK_RTDELAY) __builtin_amdgcn_s_sleep(1); }
}

}

extern "C" void kernel_launch(void* const* d_in, const int* in_sizes, int n_in, void* d_out, int out_size, void* d_ws, size_t ws_size,
                              hipStream_t stream) {
    mk::Params p;
    p.x = (const u16*)d_in[0];
    p.kc = (const u16*)d_in[1];
    p.vc = (const u16*)d_in[2];
    p.wq = (const u16*)d_in[3];
    p.wo = (const u16*)d_in[4];
    p.w1 = (const u16*)d_in[6];
    p.b1 = (const u16*)d_in[7];
    p.w2 = (const u16*)d_in[8];
    p.b2 = (const u16*)d_in[9];
    p.out = (u16*)d_out;
    p.ws = (unsigned char*)d_ws;
    mk::fused_kernel<<<256, 512, 0, stream>>>(p);
}
```

```cpp
#include <hip/hip_runtime.h>
#include <hip/hip_bf16.h>
#include <stdint.h>

#define GAS __attribute__((address_space(1)))
#define LAS __attribute__((address_space(3)))
typedef unsigned short u16;
typedef unsigned v4u __attribute__((ext_vector_type(4)));
typedef unsigned v2u __attribute__((ext_vector_type(2)));
typedef float f32x4 __attribute__((ext_vector_type(4)));
typedef float f32x2 __attribute__((ext_vector_type(2)));
typedef __bf16 bf16x8 __attribute__((ext_vector_type(8)));
typedef __bf16 bf16x2 __attribute__((ext_vector_type(2)));

#define MK_AUXNT 18
#define MK_WCH 8
#define MK_STAMPS 0
#define MK_ORDER 0
#define MK_CSLEEP 0
#define MK_DQ 16
#define MK_DO 16
#define MK_DH 16
#define MK_DF 8

namespace mk {

constexpr int B = 8, D = 1024, SK = 4096, DH = 128, WIN = 1024, F = 3584, NQ = 4096;
constexpr int NCH = 8, CH = 4, WCH = MK_WCH;

constexpr int WS_EPOCH = 0;
constexpr int QP_ROW = 16384, QP_OFF = 4096;
constexpr int HP_ROW = 14336, HP_OFF = QP_OFF + 32 * QP_ROW;
constexpr int OP_OFF = HP_OFF + 32 * HP_ROW;
constexpr int OM_OFF = OP_OFF + 32 * 8 * 8 * 512;
constexpr int SP_OFF = OM_OFF + 32 * 8 * 8 * 16;
constexpr int YP_OFF = SP_OFF + 4 * 64 * 8192;
constexpr int WS_END = YP_OFF + 4 * 56 * 8192;
constexpr int ST_OFF = WS_END;
constexpr int WS_BYTES = ST_OFF + 256 * 8 * 16 * 8;

constexpr int XN_STR = 2048 + 16, XN_OFF = 0;
constexpr int QS_STR = 256 + 16, QS_OFF = XN_OFF + 8 * XN_STR;
constexpr int AC_STR = 128 + 16, OA_OFF = QS_OFF + 8 * QS_STR;
constexpr int HA_OFF = OA_OFF + 8 * AC_STR;
constexpr int ZR_OFF = HA_OFF + 8 * AC_STR;
constexpr int KV_OFF = ZR_OFF + 128;
constexpr int ST1_OFF = KV_OFF + 4096;
constexpr int FL_OFF = ST1_OFF + 512;
constexpr int KS_OFF = FL_OFF + 256;
constexpr int LDS_USED = KS_OFF + 32768;
constexpr int LDS_BYTES = 96 * 1024;
static_assert(LDS_USED + 1024 <= LDS_BYTES && (KS_OFF % 16) == 0 && (KV_OFF % 16) == 0 && (ZR_OFF % 16) == 0, "lds");
enum { FL_PROG = 0  , FL_XN = 8, FL_Q = 9, FL_KV1 = 10, FL_O = 11, FL_H = 12, FL_T = 13, FL_TV = 14, FL_HT = 15, FL_OT = 16, FL_ST = 17, FL_QT = 18, FL_YT = 19 };

struct Params {
    const u16 *x, *kc, *vc, *wq, *wo, *w1, *b1, *w2, *b2;
    u16* out;
    unsigned char* ws;
};

constexpr unsigned kSpinCap = 1u << 16;
constexpr int AUX_NT = MK_AUXNT, AUX_SC1 = 16;

__device__ __forceinline__ float bf_lo(unsigned u) { return __builtin_bit_cast(float, u << 16); }
__device__ __forceinline__ float bf_hi(unsigned u) { return __builtin_bit_cast(float, u & 0xffff0000u); }
__device__ __forceinline__ unsigned pk_bf16(float lo, float hi) {
    f32x2 v = {lo, hi};
    return __builtin_bit_cast(unsigned, __builtin_convertvector(v, bf16x2));
}
__device__ __forceinline__ f32x4 mfma(v4u a, v4u b, f32x4 c) {
    return __builtin_amdgcn_mfma_f32_16x16x32_bf16(__builtin_bit_cast(bf16x8, a), __builtin_bit_cast(bf16x8, b), c, 0, 0, 0);
}
__device__ __forceinline__ unsigned f2u(float f) { return __builtin_bit_cast(unsigned, f); }
__device__ __forceinline__ float u2f(unsigned u) { return __builtin_bit_cast(float, u); }

#define CBAR() asm volatile("" ::: "memory")
template <int N> __device__ __forceinline__ void waitvm() { asm volatile("s_waitcnt vmcnt(%0)" ::"i"(N) : "memory"); }

__device__ __forceinline__ void lds_set(LAS unsigned* p, unsigned v) {
    CBAR();
    __hip_atomic_store(p, v, __ATOMIC_RELAXED, __HIP_MEMORY_SCOPE_WORKGROUP);
    CBAR();
}
__device__ __forceinline__ void lds_inc(LAS unsigned* p, int lane) {
    CBAR();
    if (lane == 0) __hip_atomic_fetch_add(p, 1u, __ATOMIC_RELAXED, __HIP_MEMORY_SCOPE_WORKGROUP);
    CBAR();
}
__device__ __forceinline__ unsigned lds_get(LAS unsigned* p) {
    return (unsigned)__builtin_amdgcn_readfirstlane(__hip_atomic_load(p, __ATOMIC_RELAXED, __HIP_MEMORY_SCOPE_WORKGROUP));
}
__device__ __forceinline__ void lds_wait_ge(LAS unsigned* p, unsigned v) {
#pragma nounroll
    for (unsigned spins = 0; spins < kSpinCap; ++spins) {
        if (lds_get(p) >= v) break;
        __builtin_amdgcn_s_sleep(1);
    }
    CBAR();
}

__device__ __forceinline__ float xrow16_max(float x) {
    auto s = __builtin_amdgcn_permlane16_swap(f2u(x), f2u(x), false, false);
    x = fmaxf(u2f(s[0]), u2f(s[1]));
    auto t = __builtin_amdgcn_permlane32_swap(f2u(x), f2u(x), false, false);
    return fmaxf(u2f(t[0]), u2f(t[1]));
}
__device__ __forceinline__ float xrow16_sum(float x) {
    auto s = __builtin_amdgcn_permlane16_swap(f2u(x), f2u(x), false, false);
    x = u2f(s[0]) + u2f(s[1]);
    auto t = __builtin_amdgcn_permlane32_swap(f2u(x), f2u(x), false, false);
    return u2f(t[0]) + u2f(t[1]);
}
template <int CTRL> __device__ __forceinline__ float dpp(float x) {
    return u2f(__builtin_amdgcn_mov_dpp(f2u(x), CTRL, 0xf, 0xf, true));
}
constexpr int XOR1 = 0xB1, XOR2 = 0x4E, XOR7 = 0x141, XOR8 = 0x128;
__device__ __forceinline__ float row16_sum(float x) {
    x += dpp<XOR1>(x); x += dpp<XOR2>(x); x += dpp<XOR7>(x); x += dpp<XOR8>(x);
    return x;
}
__device__ __forceinline__ float wave_sum(float x) { return xrow16_sum(row16_sum(x)); }

#define STAMP(i) do {} while (0)

__device__ __forceinline__ void transpose8(v4u* r) {
    v4u o[8];
#pragma unroll
    for (int t = 0; t < 4; ++t)
#pragma unroll
        for (int jp = 0; jp < 4; ++jp) {
            const unsigned x0 = r[2 * jp][t], x1 = r[2 * jp + 1][t];
            o[2 * t][jp] = __builtin_amdgcn_perm(x1, x0, 0x05040100u);
            o[2 * t + 1][jp] = __builtin_amdgcn_perm(x1, x0, 0x07060302u);
        }
#pragma unroll
    for (int e = 0; e < 8; ++e) { asm volatile("" : "+v"(o[e])); r[e] = o[e]; }
}

__device__ __forceinline__ void lds_wait_ge_fast(LAS unsigned* p, unsigned v) {
#pragma nounroll
    for (unsigned spins = 0; spins < 16u * kSpinCap; ++spins) {
        if (lds_get(p) >= v) break;
    }
    CBAR();
}
template <int K, class LD>
__device__ __forceinline__ void stream_head(LD& ld, v4u (&r)[32], LAS unsigned* prev) {
    if constexpr (K == 0) lds_wait_ge(prev, NCH + K - WCH > 0 ? NCH + K - WCH : 1);
    lds_wait_ge_fast(prev, NCH + K - WCH + 1);
#pragma unroll
    for (int i = 0; i < CH; ++i) r[K * CH + i] = ld(K * CH + i);
    __builtin_amdgcn_sched_barrier(0);
    if constexpr (K + 1 < WCH) stream_head<K + 1>(ld, r, prev);
}
template <int K, class LD, class ON>
__device__ __forceinline__ void stream_step(LD& ld, ON& on, v4u (&r)[32], LAS unsigned* me) {
    if constexpr (K < NCH) {
        waitvm<(WCH - 1) * CH>();
        lds_set(me, K - WCH + 1);
#pragma unroll
        for (int i = 0; i < CH; ++i) r[K * CH + i] = ld(K * CH + i);
        __builtin_amdgcn_sched_barrier(0);
        on.template run<K - WCH>(r);
        __builtin_amdgcn_sched_barrier(0);
    } else {
        waitvm<(NCH + WCH - 1 - K) * CH>();
        lds_set(me, K - WCH + 1);
        on.template run<K - WCH>(r);
        __builtin_amdgcn_sched_barrier(0);
    }
    if constexpr (K + 1 < NCH + WCH) stream_step<K + 1>(ld, on, r, me);
}

template <int N>
__device__ __forceinline__ void gather(__amdgpu_buffer_rsrc_t rs, const int (&voff)[N], unsigned tag, v4u (&g)[N]) {
    unsigned pend = (1u << N) - 1u;
#pragma nounroll
    for (unsigned spins = 0; spins < kSpinCap; ++spins) {
        CBAR();
#pragma unroll
        for (int i = 0; i < N; ++i)
            if (pend & (1u << i)) g[i] = __builtin_amdgcn_raw_buffer_load_b128(rs, voff[i], 0, AUX_SC1);
#pragma unroll
        for (int i = 0; i < N; ++i)
            if ((pend & (1u << i)) && __all(g[i].y == tag && g[i].w == tag)) pend &= ~(1u << i);
        if (pend == 0u) break;
    }
    CBAR();
}

struct GemmOn {
    LAS unsigned* flag; unsigned need;
    const LAS unsigned char* pa0; const LAS unsigned char* pa1; int step;
    f32x4 (&acc)[8];
    v4u (&a)[4];
    int done;
    __device__ __forceinline__ void load_a() {
        a[0] = *(const LAS v4u*)(pa0); a[1] = *(const LAS v4u*)(pa1);
        a[2] = *(const LAS v4u*)(pa0 + step); a[3] = *(const LAS v4u*)(pa1 + step);
    }
    template <int G> __device__ __forceinline__ void mul(v4u (&r)[32]) {
#pragma unroll
        for (int e = 0; e < 8; ++e) acc[e] = mfma(a[G], r[8 * G + e], acc[e]);
    }
    template <int C> __device__ __forceinline__ void run(v4u (&r)[32]) {
        if constexpr (C & 1) {
            constexpr int G = C >> 1;
            transpose8(&r[8 * G]);
            if (done < 0 && lds_get(flag) >= need) {
                CBAR();
                load_a();
                done = 0;
                if constexpr (G >= 1) mul<0>(r);
                if constexpr (G >= 2) mul<1>(r);
                if constexpr (G >= 3) mul<2>(r);
            }
            if (done >= 0) mul<G>(r);
        }
    }
    __device__ __forceinline__ void finish(v4u (&r)[32]) {
        if (done < 0) {
            lds_wait_ge(flag, need);
            load_a();
            mul<0>(r); mul<1>(r); mul<2>(r); mul<3>(r);
        }
    }
};
struct KvOn {
    LAS unsigned char* ks; int lane;
    template <int C> __device__ __forceinline__ void run(v4u (&r)[32]) {
        if constexpr (C == 1 || C == 3) {
            constexpr int H = C >> 1;
            const int g = lane >> 4, c = lane & 15;
#pragma unroll
            for (int i = 0; i < 8; ++i) {
                const int key = 32 * H + 4 * i + g;
                *(LAS v4u*)(ks + key * 256 + ((c ^ ((4 * i + g) & 15)) * 16)) = r[8 * H + i];
            }
            CBAR();
#pragma unroll
            for (int t = 0; t < 2; ++t)
#pragma unroll
                for (int s = 0; s < 4; ++s) r[8 * H + 4 * t + s] = *(const LAS v4u*)(ks + (32 * H + 16 * t + c) * 256 + (((4 * s + g) ^ c) * 16));
        }
        if constexpr (C == 5) transpose8(&r[16]);
        if constexpr (C == 7) transpose8(&r[24]);
    }
};

__device__ __forceinline__ void fold_halves(const f32x4 (&acc)[8], int lane, float (&res)[8]) {
    const bool lo = (lane & 8) == 0;
#pragma unroll
    for (int e = 0; e < 8; ++e) {
        auto s = __builtin_amdgcn_permlane32_swap(f2u(acc[e][0]), f2u(acc[e][1]), false, false);
        auto t = __builtin_amdgcn_permlane32_swap(f2u(acc[e][2]), f2u(acc[e][3]), false, false);
        float s1 = u2f(s[0]) + dpp<XOR8>(u2f(s[1]));
        float s2 = u2f(t[1]) + dpp<XOR8>(u2f(t[0]));
        asm volatile("" : "+v"(s1), "+v"(s2));
        res[e] = lo ? s1 : s2;
    }
}

__device__ __forceinline__ void ln_rows(const v4u (&xv)[4], LAS unsigned char* dst, int lane) {
    float v[2][16], mean[2], rstd[2];
#pragma unroll
    for (int k = 0; k < 2; ++k) {
#pragma unroll
        for (int q = 0; q < 4; ++q) {
            v[k][2 * q] = bf_lo(xv[2 * k][q]); v[k][2 * q + 1] = bf_hi(xv[2 * k][q]);
            v[k][8 + 2 * q] = bf_lo(xv[2 * k + 1][q]); v[k][8 + 2 * q + 1] = bf_hi(xv[2 * k + 1][q]);
        }
        float s4[4];
#pragma unroll
        for (int q = 0; q < 4; ++q) s4[q] = (v[k][4 * q] + v[k][4 * q + 1]) + (v[k][4 * q + 2] + v[k][4 * q + 3]);
        mean[k] = (s4[0] + s4[1]) + (s4[2] + s4[3]);
    }
    mean[0] = wave_sum(mean[0]) * (1.0f / D);
    mean[1] = wave_sum(mean[1]) * (1.0f / D);
#pragma unroll
    for (int k = 0; k < 2; ++k) {
#pragma unroll
        for (int i = 0; i < 16; ++i) v[k][i] -= mean[k];
        float s4[4];
#pragma unroll
        for (int q = 0; q < 4; ++q)
            s4[q] = fmaf(v[k][4 * q], v[k][4 * q], v[k][4 * q + 1] * v[k][4 * q + 1]) + fmaf(v[k][4 * q + 2], v[k][4 * q + 2], v[k][4 * q + 3] * v[k][4 * q + 3]);
        rstd[k] = (s4[0] + s4[1]) + (s4[2] + s4[3]);
    }
    rstd[0] = wave_sum(rstd[0]);
    rstd[1] = wave_sum(rstd[1]);
#pragma unroll
    for (int k = 0; k < 2; ++k) {
        const float rs_ = 1.0f / sqrtf(rstd[k] * (1.0f / D) + 1e-5f);
#pragma unroll
        for (int h = 0; h < 2; ++h) {
            const v4u o = {pk_bf16(v[k][h * 8] * rs_, v[k][h * 8 + 1] * rs_), pk_bf16(v[k][h * 8 + 2] * rs_, v[k][h * 8 + 3] * rs_),
                           pk_bf16(v[k][h * 8 + 4] * rs_, v[k][h * 8 + 5] * rs_), pk_bf16(v[k][h * 8 + 6] * rs_, v[k][h * 8 + 7] * rs_)};
            *(LAS v4u*)(dst + k * XN_STR + h * 1024 + lane * 16) = o;
        }
    }
}
__device__ __forceinline__ void load_x_rows(const u16* x, int row0, int lane, v4u (&xv)[4]) {
    const GAS unsigned char* xp = (const GAS unsigned char*)x + row0 * 2048 + lane * 16;
#pragma unroll
    for (int i = 0; i < 4; ++i) xv[i] = *(const GAS v4u*)(xp + i * 1024);
}

__device__ __forceinline__ float gelu_tanh(float s) {
    const float u = 0.7978845608028654f * (s + 0.044715f * s * s * s);
    const float e = __expf(2.0f * u);
    const float th = 1.0f - 2.0f / (e + 1.0f);
    return 0.5f * s * (1.0f + th);
}
__device__ __forceinline__ float silu(float y) { return y / (1.0f + __expf(-y)); }

__global__ void __launch_bounds__(512, 2) fused_kernel(Params p) {
    __shared__ __attribute__((aligned(16))) unsigned char smem[LDS_BYTES];
    LAS unsigned char* L = (LAS unsigned char*)smem;
    LAS unsigned* FL = (LAS unsigned*)(L + FL_OFF);
    const int tid = threadIdx.x, lane = tid & 63, w = __builtin_amdgcn_readfirstlane(tid >> 6), bid = blockIdx.x;
    const int g = lane >> 4, c = lane & 15;
    const __amdgpu_buffer_rsrc_t rs = __builtin_amdgcn_make_buffer_rsrc((void*)p.ws, 0, WS_BYTES, 0x00020000);
    STAMP(0);
    constexpr float C2 = 0.08838834764831845f * 1.4426950408889634f;

    const int xcd = bid & 7, idx = bid >> 3;
    const bool has_w1 = bid < 224;
    const bool has_w2 = idx < 28;
    const int ks_o = xcd * 8 + (idx >> 2), nsl_o = idx & 3;
    const int t2 = xcd * 28 + idx, ks_2 = t2 >> 2, nsl_2 = t2 & 3;
    int prev;
    if (MK_ORDER == 0) prev = (w == 4 && !has_w1) ? 2 : w - 1;
    else prev = w == 3 ? 0 : w == 1 ? (has_w1 ? 3 : 0) : w == 5 ? 2 : w == 4 ? (has_w2 ? 5 : 2) : w - 1;

    const bool is_gemm = (w == 0 || w == 3 || w == 4 || w == 5);
    if (is_gemm) {
        v4u r[32];
        const bool ta = (w == 0 || w == 3);
        const u16* W = w == 0 ? p.wq : w == 3 ? p.w1 : w == 4 ? p.wo : p.w2;
        const int ldw = w == 0 ? NQ * 2 : w == 3 ? F * 2 : D * 2;
        const int nbytes = w == 3 || w == 5 ? D * F * 2 : D * NQ * 2;
        const int ks = ta ? (bid & 3) : w == 4 ? ks_o : ks_2;
        const int ns = ta ? (bid >> 2) : w == 4 ? nsl_o : nsl_2;
        const __amdgpu_buffer_rsrc_t rw = __builtin_amdgcn_make_buffer_rsrc((void*)W, 0, nbytes, 0x00020000);
        const int voffw = ta ? (ks * 256 + 32 * (c >> 3) + 8 * g) * ldw + (ns * 64 + 8 * (c & 7)) * 2
                             : (ks * 64 + 8 * g) * ldw + (ns * 256 + 8 * c) * 2;
        const int go1 = ta ? 64 * ldw : 256, go2 = ta ? 128 * ldw : 32 * ldw;
        auto ldg = [&](int i) {
            return __builtin_amdgcn_raw_buffer_load_b128(rw, voffw, (i & 7) * ldw + ((i >> 3) & 1) * go1 + (i >> 4) * go2, AUX_NT);
        };
        if (w == 0) {
            FL[lane] = 0u;
            if (lane < 32) ((LAS unsigned*)(L + ZR_OFF))[lane] = 0u;
#pragma unroll
            for (int i = 0; i < WCH * CH; ++i) r[i] = ldg(i);
            __builtin_amdgcn_sched_barrier(0);
            __syncthreads();
        } else {
            v4u xv[4];
            if (w >= 4) load_x_rows(p.x, (w - 4) * 2, lane, xv);
            __syncthreads();
            if (w >= 4) { ln_rows(xv, L + XN_OFF + (w - 4) * 2 * XN_STR, lane); lds_inc(FL + FL_XN, lane); STAMP(8); }
        }
        const bool has = w == 3 ? has_w1 : w == 5 ? has_w2 : true;
        LAS unsigned* trig = FL + (w == 0 ? FL_QT : w == 3 ? FL_HT : w == 4 ? FL_ST : FL_YT);
        if (has) {
            unsigned bbias = 0;
            if (w != 0) stream_head<0>(ldg, r, FL + FL_PROG + prev);
            f32x4 acc[8];
#pragma unroll
            for (int e = 0; e < 8; ++e) acc[e] = f32x4{0.f, 0.f, 0.f, 0.f};
            v4u a[4];
            const LAS unsigned char* pa0; const LAS unsigned char* pa1; int step;
            if (ta) {
                pa0 = L + XN_OFF + ks * 512 + (c & 7) * XN_STR + (32 * (c >> 3) + 8 * g) * 2; pa1 = pa0 + 128; step = 256;
            } else {
                const LAS unsigned char* act = L + (w == 4 ? OA_OFF : HA_OFF) + (c & 7) * AC_STR + g * 16;
                const LAS unsigned char* zr = L + ZR_OFF + g * 16;
                pa0 = c < 8 ? act : zr; pa1 = c < 8 ? zr : act; step = 64;
            }
            GemmOn on{FL + (ta ? FL_XN : w == 4 ? FL_O : FL_H), ta ? 4u : w == 4 ? 2u : 1u, pa0, pa1, step, acc, a, -1};
            stream_step<WCH>(ldg, on, r, FL + FL_PROG + w);
            STAMP(2);
            on.finish(r);
            STAMP(3);
            lds_wait_ge(FL + FL_T, 1);
            const unsigned tag = __builtin_amdgcn_readfirstlane(FL[FL_TV]);
            __builtin_amdgcn_s_setprio(3);
            if (ta) {
                float res[8];
                fold_halves(acc, lane, res);
                const int b = 4 * (g & 1) + (g >> 1) + 2 * (c >> 3);
                const int o = (w == 0 ? QP_OFF + (ks * 8 + b) * QP_ROW : HP_OFF + (ks * 8 + b) * HP_ROW) + ns * 256 + (c & 7) * 16;
#pragma unroll
                for (int u = 0; u < 2; ++u)
                    __builtin_amdgcn_raw_buffer_store_b128(v4u{pk_bf16(res[4 * u], res[4 * u + 1]), tag, pk_bf16(res[4 * u + 2], res[4 * u + 3]), tag}, rs, o, u * 128, AUX_SC1);
            } else {
                const int o = (w == 4 ? SP_OFF + (ns * 64 + ks) * 8192 : YP_OFF + (ns * 56 + ks) * 8192) + lane * 16;
#pragma unroll
                for (int rr = 0; rr < 4; ++rr)
#pragma unroll
                    for (int eh = 0; eh < 2; ++eh)
                        __builtin_amdgcn_raw_buffer_store_b128(
                            v4u{pk_bf16(acc[4 * eh][rr], acc[4 * eh + 1][rr]), tag, pk_bf16(acc[4 * eh + 2][rr], acc[4 * eh + 3][rr]), tag}, rs, o,
                            (2 * rr + eh) * 1024, AUX_SC1);
            }
            lds_set(trig, 1);
            STAMP(4);
            (void)bbias;
        } else {
            if (w == 3) lds_wait_ge(FL + FL_PROG + (MK_ORDER == 0 ? 4 : 1), NCH / 2);
            else lds_wait_ge(FL + FL_PROG + 4, NCH);
            lds_set(trig, 1);
        }
        if (w == 3 && has_w2) {
            lds_wait_ge(FL + FL_T, 1);
            const unsigned tag = __builtin_amdgcn_readfirstlane(FL[FL_TV]);
            const int n4 = 8 * (c & 7) + 4 * (c >> 3);
            const v2u bb = *(const GAS v2u*)(p.b1 + ks_2 * 64 + n4);
            int vo[8];
#pragma unroll
            for (int i = 0; i < 8; ++i) vo[i] = HP_OFF + ((i >> 1) * 8 + (i & 1) * 4 + g) * HP_ROW + ks_2 * 256 + c * 16;
            __builtin_amdgcn_s_sleep(MK_DH);
            v4u h8[8];
            gather<8>(rs, vo, tag, h8);
            STAMP(5);
#pragma unroll
            for (int bh = 0; bh < 2; ++bh) {
                float v0 = (bf_lo(h8[bh].x) + bf_lo(h8[2 + bh].x)) + (bf_lo(h8[4 + bh].x) + bf_lo(h8[6 + bh].x)) + bf_lo(bb.x);
                float v1 = (bf_hi(h8[bh].x) + bf_hi(h8[2 + bh].x)) + (bf_hi(h8[4 + bh].x) + bf_hi(h8[6 + bh].x)) + bf_hi(bb.x);
                float v2 = (bf_lo(h8[bh].z) + bf_lo(h8[2 + bh].z)) + (bf_lo(h8[4 + bh].z) + bf_lo(h8[6 + bh].z)) + bf_lo(bb.y);
                float v3 = (bf_hi(h8[bh].z) + bf_hi(h8[2 + bh].z)) + (bf_hi(h8[4 + bh].z) + bf_hi(h8[6 + bh].z)) + bf_hi(bb.y);
                *(LAS v2u*)(L + HA_OFF + (bh * 4 + g) * AC_STR + n4 * 2) = v2u{pk_bf16(gelu_tanh(v0), gelu_tanh(v1)), pk_bf16(gelu_tanh(v2), gelu_tanh(v3))};
            }
            lds_set(FL + FL_H, 1);
            STAMP(6);
        }
    } else if (w == 1 || w == 2) {
        const int gid = xcd * 4 + (idx >> 3), sp = idx & 7;
        const size_t kvbase = ((size_t)gid * SK + (SK - WIN) + sp * 128 + (w - 1) * 64) * (DH * 2);
        const __amdgpu_buffer_rsrc_t rk = __builtin_amdgcn_make_buffer_rsrc((void*)((const unsigned char*)p.kc + kvbase), 0, 64 * 256, 0x00020000);
        const __amdgpu_buffer_rsrc_t rv = __builtin_amdgcn_make_buffer_rsrc((void*)((const unsigned char*)p.vc + kvbase), 0, 64 * 256, 0x00020000);
        const int vvo = g * 1024 + c * 16;
        auto ld = [&](int i) {
            if (i < 16) return __builtin_amdgcn_raw_buffer_load_b128(rk, lane * 16, i * 1024, AUX_NT);
            const int j = i - 16;
            return __builtin_amdgcn_raw_buffer_load_b128(rv, vvo, (j >> 3) * 8192 + ((j >> 2) & 1) * 4096 + (j & 3) * 256, AUX_NT);
        };
        KvOn on{L + KS_OFF + (w - 1) * 16384, lane};
        v4u r[32];
        __syncthreads();
        stream_head<0>(ld, r, FL + FL_PROG + prev);
        stream_step<WCH>(ld, on, r, FL + FL_PROG + w);
        STAMP(2);
        lds_wait_ge(FL + FL_Q, 2);
        STAMP(3);
        __builtin_amdgcn_s_setprio(3);
        v4u qf[4];
#pragma unroll
        for (int s = 0; s < 4; ++s) qf[s] = *(const LAS v4u*)(L + QS_OFF + (c & 7) * QS_STR + (32 * s + 8 * g) * 2);
        f32x4 S[4];
#pragma unroll
        for (int t = 0; t < 4; ++t) S[t] = f32x4{0.f, 0.f, 0.f, 0.f};
#pragma unroll
        for (int s = 0; s < 4; ++s)
#pragma unroll
            for (int t = 0; t < 4; ++t) S[t] = mfma(r[4 * t + s], qf[s], S[t]);
        float mx = fmaxf(fmaxf(fmaxf(S[0][0], S[0][1]), fmaxf(S[0][2], S[0][3])), fmaxf(fmaxf(S[1][0], S[1][1]), fmaxf(S[1][2], S[1][3])));
        mx = fmaxf(mx, fmaxf(fmaxf(fmaxf(S[2][0], S[2][1]), fmaxf(S[2][2], S[2][3])), fmaxf(fmaxf(S[3][0], S[3][1]), fmaxf(S[3][2], S[3][3]))));
        mx = xrow16_max(mx);
        LAS float* st1 = (LAS float*)(L + ST1_OFF);
        f32x4 O[4];
        float lsum = 0.f, m = mx;
        if (w == 2) {
            lds_wait_ge(FL + FL_KV1, 1);
            const float m1 = st1[c & 7], l1 = st1[8 + (c & 7)];
            m = fmaxf(mx, m1);
            const float f1 = __builtin_amdgcn_exp2f((m1 - m) * C2);
            lsum = (g == 0) ? f1 * l1 : 0.f;
            if (lane < 8) st1[16 + lane] = f1;
            CBAR();
            const f32x4 f4 = *(const LAS f32x4*)(st1 + 16 + 4 * (g & 1));
#pragma unroll
            for (int e = 0; e < 4; ++e) O[e] = *(const LAS f32x4*)(L + KV_OFF + (e * 64 + lane) * 16) * f4;
        } else {
#pragma unroll
            for (int e = 0; e < 4; ++e) O[e] = f32x4{0.f, 0.f, 0.f, 0.f};
        }
        const float mc = m * C2;
        float ls4[4];
#pragma unroll
        for (int t = 0; t < 4; ++t) {
#pragma unroll
            for (int q = 0; q < 4; ++q) S[t][q] = __builtin_amdgcn_exp2f(fmaf(S[t][q], C2, -mc));
            ls4[t] = (S[t][0] + S[t][1]) + (S[t][2] + S[t][3]);
        }
        lsum += (ls4[0] + ls4[1]) + (ls4[2] + ls4[3]);
        const bool lo8 = c < 8;
#pragma unroll
        for (int u = 0; u < 2; ++u) {
            const v4u pa = {pk_bf16(S[2 * u][0], S[2 * u][1]), pk_bf16(S[2 * u][2], S[2 * u][3]),
                            pk_bf16(S[2 * u + 1][0], S[2 * u + 1][1]), pk_bf16(S[2 * u + 1][2], S[2 * u + 1][3])};
            const v4u z = {0u, 0u, 0u, 0u};
            const v4u p0 = lo8 ? pa : z, p1 = lo8 ? z : pa;
#pragma unroll
            for (int e = 0; e < 4; ++e) O[e] = mfma(p0, r[16 + 8 * u + e], O[e]);
#pragma unroll
            for (int e = 0; e < 4; ++e) O[e] = mfma(p1, r[16 + 8 * u + 4 + e], O[e]);
        }
        lsum = xrow16_sum(lsum);
        if (w == 1) {
#pragma unroll
            for (int e = 0; e < 4; ++e) *(LAS f32x4*)(L + KV_OFF + (e * 64 + lane) * 16) = O[e];
            if (lane < 8) { st1[lane] = m; st1[8 + lane] = lsum; }
            lds_set(FL + FL_KV1, 1);
            STAMP(4);
        } else {
            const unsigned tag = __builtin_amdgcn_readfirstlane(FL[FL_TV]);
            const int obase = OP_OFF + ((gid * 8 + sp) * 8 + 4 * (g & 1)) * 512 + (g >> 1) * 256 + c * 16;
#pragma unroll
            for (int rr = 0; rr < 4; ++rr)
                __builtin_amdgcn_raw_buffer_store_b128(v4u{pk_bf16(O[0][rr], O[1][rr]), tag, pk_bf16(O[2][rr], O[3][rr]), tag}, rs, obase, rr * 512, AUX_SC1);
            if (lane < 8) __builtin_amdgcn_raw_buffer_store_b128(v4u{f2u(m), tag, f2u(lsum), tag}, rs, OM_OFF + ((gid * 8 + lane) * 8 + sp) * 16, 0, AUX_SC1);
            lds_set(FL + FL_OT, 1);
            STAMP(4);
        }
    } else {
        const int hw = w - 6;
        unsigned ep = 0;
        if (hw == 1) ep = __hip_atomic_load((GAS unsigned*)(p.ws + WS_EPOCH), __ATOMIC_RELAXED, __HIP_MEMORY_SCOPE_AGENT);
        unsigned touch = 0;
        {
            v4u xv[4];
            load_x_rows(p.x, 4 + hw * 2, lane, xv);
            __syncthreads();
            if (hw == 1) {
                FL[FL_TV] = __builtin_amdgcn_readfirstlane(ep) + 1u;
                lds_set(FL + FL_T, 1);
            }
            ln_rows(xv, L + XN_OFF + (4 + hw * 2) * XN_STR, lane);
            lds_inc(FL + FL_XN, lane);
            STAMP(8);
            asm volatile("" :: "v"(touch));
        }
        __builtin_amdgcn_s_setprio(3);
        lds_wait_ge(FL + FL_T, 1);
        const unsigned tag = __builtin_amdgcn_readfirstlane(FL[FL_TV]);
        {
            const int gid = xcd * 4 + (idx >> 3), bq = gid >> 2, hk = gid & 3;
            int vo[8];
#pragma unroll
            for (int i = 0; i < 8; ++i) vo[i] = QP_OFF + ((i >> 1) * 8 + bq) * QP_ROW + (hk * 16 + hw * 8 + (i & 1) * 4 + g) * 256 + c * 16;
            lds_wait_ge(FL + FL_QT, 1);
            __builtin_amdgcn_s_sleep(MK_DQ);
            v4u q8[8];
            gather<8>(rs, vo, tag, q8);
            STAMP(3);
            const int n4 = 8 * (c & 7) + 4 * (c >> 3);
#pragma unroll
            for (int sh = 0; sh < 2; ++sh) {
                const float v0 = (bf_lo(q8[sh].x) + bf_lo(q8[2 + sh].x)) + (bf_lo(q8[4 + sh].x) + bf_lo(q8[6 + sh].x));
                const float v1 = (bf_hi(q8[sh].x) + bf_hi(q8[2 + sh].x)) + (bf_hi(q8[4 + sh].x) + bf_hi(q8[6 + sh].x));
                const float v2 = (bf_lo(q8[sh].z) + bf_lo(q8[2 + sh].z)) + (bf_lo(q8[4 + sh].z) + bf_lo(q8[6 + sh].z));
                const float v3 = (bf_hi(q8[sh].z) + bf_hi(q8[2 + sh].z)) + (bf_hi(q8[4 + sh].z) + bf_hi(q8[6 + sh].z));
                const int n = (hw * 8 + sh * 4 + g) * 64 + n4;
                *(LAS v2u*)(L + QS_OFF + (n >> 7) * QS_STR + (n & 127) * 2) = v2u{pk_bf16(v0, v1), pk_bf16(v2, v3)};
            }
            lds_inc(FL + FL_Q, lane);
            STAMP(4);
        }
        {
            const int head = ks_o >> 1, dh = ks_o & 1, hk = head >> 3, hh = head & 7;
            const int gidb = (4 * hw + g) * 4 + hk;
            int vo[9];
#pragma unroll
            for (int i = 0; i < 8; ++i) vo[i] = OP_OFF + ((gidb * 8 + i) * 8 + hh) * 512 + (c >> 3) * 256 + (8 * dh + (c & 7)) * 16;
            vo[8] = OM_OFF + ((gidb * 8 + hh) * 8 + (c & 7)) * 16;
            lds_wait_ge(FL + FL_OT, 1);
            __builtin_amdgcn_s_sleep(MK_DO);
            v4u o9[9];
            gather<9>(rs, vo, tag, o9);
            STAMP(5);
            const float ms = u2f(o9[8].x), ls = u2f(o9[8].z);
            float M = ms;
            M = fmaxf(M, dpp<XOR1>(M)); M = fmaxf(M, dpp<XOR2>(M)); M = fmaxf(M, dpp<XOR7>(M));
            const float we = __builtin_amdgcn_exp2f((ms - M) * C2);
            float Ls = we * ls;
            Ls += dpp<XOR1>(Ls); Ls += dpp<XOR2>(Ls); Ls += dpp<XOR7>(Ls);
            const float wn = we / Ls;
            LAS float* wl = (LAS float*)(L + ST1_OFF + 128) + hw * 32 + g * 8;
            if (c < 8) wl[c] = wn;
            CBAR();
            const f32x4 wa = *(const LAS f32x4*)wl, wb = *(const LAS f32x4*)(wl + 4);
            float v0 = 0.f, v1 = 0.f, v2 = 0.f, v3 = 0.f;
#pragma unroll
            for (int i = 0; i < 8; ++i) {
                const float wi = i < 4 ? wa[i & 3] : wb[i & 3];
                v0 = fmaf(wi, bf_lo(o9[i].x), v0); v1 = fmaf(wi, bf_hi(o9[i].x), v1);
                v2 = fmaf(wi, bf_lo(o9[i].z), v2); v3 = fmaf(wi, bf_hi(o9[i].z), v3);
            }
            *(LAS v2u*)(L + OA_OFF + (4 * hw + g) * AC_STR + (8 * (c & 7) + 4 * (c >> 3)) * 2) = v2u{pk_bf16(v0, v1), pk_bf16(v2, v3)};
            lds_inc(FL + FL_O, lane);
            STAMP(6);
        }
        if (hw == 1) {
            const int nsl = bid & 3, j = bid >> 2, st = j >> 3, G = (j & 7) >> 1, lam = lane & 7, cc = 8 * (j & 1) + lam, src = lane >> 3;
            const int b = 4 * (G & 1) + (st >> 1), n = 256 * nsl + 128 * (G >> 1) + 8 * cc + 4 * (st & 1);
            const v2u xx = *(const GAS v2u*)(p.x + b * D + n), bb = *(const GAS v2u*)(p.b2 + n);
            const int uo = (st * 64 + 8 * (j & 7) + lam) * 16;
            float sv[4], yv[4];
            {
                int vo[8];
#pragma unroll
                for (int i = 0; i < 8; ++i) vo[i] = SP_OFF + (nsl * 64 + 8 * i + src) * 8192 + uo;
                lds_wait_ge(FL + FL_ST, 1);
                __builtin_amdgcn_s_sleep(MK_DF);
                v4u f8[8];
                gather<8>(rs, vo, tag, f8);
                sv[0] = sv[1] = sv[2] = sv[3] = 0.f;
#pragma unroll
                for (int i = 0; i < 8; ++i) { sv[0] += bf_lo(f8[i].x); sv[1] += bf_hi(f8[i].x); sv[2] += bf_lo(f8[i].z); sv[3] += bf_hi(f8[i].z); }
#pragma unroll
                for (int q = 0; q < 4; ++q) { sv[q] += dpp<XOR8>(sv[q]); sv[q] = xrow16_sum(sv[q]); }
            }
            STAMP(7);
            {
                int vo[7];
#pragma unroll
                for (int i = 0; i < 7; ++i) vo[i] = YP_OFF + (nsl * 56 + 8 * i + src) * 8192 + uo;
                lds_wait_ge(FL + FL_YT, 1);
                __builtin_amdgcn_s_sleep(MK_DF);
                v4u f7[7];
                gather<7>(rs, vo, tag, f7);
                yv[0] = yv[1] = yv[2] = yv[3] = 0.f;
#pragma unroll
                for (int i = 0; i < 7; ++i) { yv[0] += bf_lo(f7[i].x); yv[1] += bf_hi(f7[i].x); yv[2] += bf_lo(f7[i].z); yv[3] += bf_hi(f7[i].z); }
#pragma unroll
                for (int q = 0; q < 4; ++q) { yv[q] += dpp<XOR8>(yv[q]); yv[q] = xrow16_sum(yv[q]); }
            }
            const float o0 = bf_lo(xx.x) + sv[0] * silu(yv[0] + bf_lo(bb.x)), o1 = bf_hi(xx.x) + sv[1] * silu(yv[1] + bf_hi(bb.x));
            const float o2 = bf_lo(xx.y) + sv[2] * silu(yv[2] + bf_lo(bb.y)), o3 = bf_hi(xx.y) + sv[3] * silu(yv[3] + bf_hi(bb.y));
            if (lane < 8) *(GAS v2u*)(p.out + b * D + n) = v2u{pk_bf16(o0, o1), pk_bf16(o2, o3)};
            if (bid == 0 && lane == 0) __hip_atomic_store((GAS unsigned*)(p.ws + WS_EPOCH), tag, __ATOMIC_RELAXED, __HIP_MEMORY_SCOPE_AGENT);
            STAMP(1);
        }
    }
}

}

extern "C" void kernel_launch(void* const* d_in, const int* in_sizes, int n_in, void* d_out, int out_size, void* d_ws, size_t ws_size,
                              hipStream_t stream) {
    mk::Params p;
    p.x = (const u16*)d_in[0];
    p.kc = (const u16*)d_in[1];
    p.vc = (const u16*)d_in[2];
    p.wq = (const u16*)d_in[3];
    p.wo = (const u16*)d_in[4];
    p.w1 = (const u16*)d_in[6];
    p.b1 = (const u16*)d_in[7];
    p.w2 = (const u16*)d_in[8];
    p.b2 = (const u16*)d_in[9];
    p.out = (u16*)d_out;
    p.ws = (unsigned char*)d_ws;
    mk::fused_kernel<<<256, 512, 0, stream>>>(p);
}
```

```cpp
#include <hip/hip_runtime.h>
#include <hip/hip_bf16.h>
#include <stdint.h>

#define GAS __attribute__((address_space(1)))
#define LAS __attribute__((address_space(3)))
typedef unsigned short u16;
typedef unsigned v4u __attribute__((ext_vector_type(4)));
typedef unsigned v2u __attribute__((ext_vector_type(2)));
typedef float f32x4 __attribute__((ext_vector_type(4)));
typedef float f32x2 __attribute__((ext_vector_type(2)));
typedef __bf16 bf16x8 __attribute__((ext_vector_type(8)));
typedef __bf16 bf16x2 __attribute__((ext_vector_type(2)));

#define MK_FIXDUR 4000
#define MK_WCH 8
#define MK_AUXNT 0
#define MK_STAMPS 0
#define MK_ORDER 0
#define MK_CSLEEP 0
#define MK_DQ 16
#define MK_DO 16
#define MK_DH 16
#define MK_DF 8

namespace mk {

constexpr int B = 8, D = 1024, SK = 4096, DH = 128, WIN = 1024, F = 3584, NQ = 4096;
constexpr int NCH = 8, CH = 4, WCH = MK_WCH;

constexpr int WS_EPOCH = 0;
constexpr int QP_ROW = 16384, QP_OFF = 4096;
constexpr int HP_ROW = 14336, HP_OFF = QP_OFF + 32 * QP_ROW;
constexpr int OP_OFF = HP_OFF + 32 * HP_ROW;
constexpr int OM_OFF = OP_OFF + 32 * 8 * 8 * 512;
constexpr int SP_OFF = OM_OFF + 32 * 8 * 8 * 16;
constexpr int YP_OFF = SP_OFF + 4 * 64 * 8192;
constexpr int WS_END = YP_OFF + 4 * 56 * 8192;
constexpr int ST_OFF = WS_END;
constexpr int WS_BYTES = ST_OFF + 256 * 8 * 16 * 8;

constexpr int XN_STR = 2048 + 16, XN_OFF = 0;
constexpr int QS_STR = 256 + 16, QS_OFF = XN_OFF + 8 * XN_STR;
constexpr int AC_STR = 128 + 16, OA_OFF = QS_OFF + 8 * QS_STR;
constexpr int HA_OFF = OA_OFF + 8 * AC_STR;
constexpr int ZR_OFF = HA_OFF + 8 * AC_STR;
constexpr int KV_OFF = ZR_OFF + 128;
constexpr int ST1_OFF = KV_OFF + 4096;
constexpr int FL_OFF = ST1_OFF + 512;
constexpr int KS_OFF = FL_OFF + 256;
constexpr int LDS_USED = KS_OFF + 32768;
constexpr int LDS_BYTES = 96 * 1024;
static_assert(LDS_USED + 1024 <= LDS_BYTES && (KS_OFF % 16) == 0 && (KV_OFF % 16) == 0 && (ZR_OFF % 16) == 0, "lds");
enum { FL_PROG = 0  , FL_XN = 8, FL_Q = 9, FL_KV1 = 10, FL_O = 11, FL_H = 12, FL_T = 13, FL_TV = 14, FL_HT = 15, FL_OT = 16, FL_ST = 17, FL_QT = 18, FL_YT = 19 };

struct Params {
    const u16 *x, *kc, *vc, *wq, *wo, *w1, *b1, *w2, *b2;
    u16* out;
    unsigned char* ws;
};

constexpr unsigned kSpinCap = 1u << 16;
constexpr int AUX_NT = MK_AUXNT, AUX_SC1 = 16;

__device__ __forceinline__ float bf_lo(unsigned u) { return __builtin_bit_cast(float, u << 16); }
__device__ __forceinline__ float bf_hi(unsigned u) { return __builtin_bit_cast(float, u & 0xffff0000u); }
__device__ __forceinline__ unsigned pk_bf16(float lo, float hi) {
    f32x2 v = {lo, hi};
    return __builtin_bit_cast(unsigned, __builtin_convertvector(v, bf16x2));
}
__device__ __forceinline__ f32x4 mfma(v4u a, v4u b, f32x4 c) {
    return __builtin_amdgcn_mfma_f32_16x16x32_bf16(__builtin_bit_cast(bf16x8, a), __builtin_bit_cast(bf16x8, b), c, 0, 0, 0);
}
__device__ __forceinline__ unsigned f2u(float f) { return __builtin_bit_cast(unsigned, f); }
__device__ __forceinline__ float u2f(unsigned u) { return __builtin_bit_cast(float, u); }

#define CBAR() asm volatile("" ::: "memory")
template <int N> __device__ __forceinline__ void waitvm() { asm volatile("s_waitcnt vmcnt(%0)" ::"i"(N) : "memory"); }

__device__ __forceinline__ void lds_set(LAS unsigned* p, unsigned v) {
    CBAR();
    __hip_atomic_store(p, v, __ATOMIC_RELAXED, __HIP_MEMORY_SCOPE_WORKGROUP);
    CBAR();
}
__device__ __forceinline__ void lds_inc(LAS unsigned* p, int lane) {
    CBAR();
    if (lane == 0) __hip_atomic_fetch_add(p, 1u, __ATOMIC_RELAXED, __HIP_MEMORY_SCOPE_WORKGROUP);
    CBAR();
}
__device__ __forceinline__ unsigned lds_get(LAS unsigned* p) {
    return (unsigned)__builtin_amdgcn_readfirstlane(__hip_atomic_load(p, __ATOMIC_RELAXED, __HIP_MEMORY_SCOPE_WORKGROUP));
}
__device__ __forceinline__ void lds_wait_ge(LAS unsigned* p, unsigned v) {
#pragma nounroll
    for (unsigned spins = 0; spins < kSpinCap; ++spins) {
        if (lds_get(p) >= v) break;
        __builtin_amdgcn_s_sleep(1);
    }
    CBAR();
}

__device__ __forceinline__ float xrow16_max(float x) {
    auto s = __builtin_amdgcn_permlane16_swap(f2u(x), f2u(x), false, false);
    x = fmaxf(u2f(s[0]), u2f(s[1]));
    auto t = __builtin_amdgcn_permlane32_swap(f2u(x), f2u(x), false, false);
    return fmaxf(u2f(t[0]), u2f(t[1]));
}
__device__ __forceinline__ float xrow16_sum(float x) {
    auto s = __builtin_amdgcn_permlane16_swap(f2u(x), f2u(x), false, false);
    x = u2f(s[0]) + u2f(s[1]);
    auto t = __builtin_amdgcn_permlane32_swap(f2u(x), f2u(x), false, false);
    return u2f(t[0]) + u2f(t[1]);
}
template <int CTRL> __device__ __forceinline__ float dpp(float x) {
    return u2f(__builtin_amdgcn_mov_dpp(f2u(x), CTRL, 0xf, 0xf, true));
}
constexpr int XOR1 = 0xB1, XOR2 = 0x4E, XOR7 = 0x141, XOR8 = 0x128;
__device__ __forceinline__ float row16_sum(float x) {
    x += dpp<XOR1>(x); x += dpp<XOR2>(x); x += dpp<XOR7>(x); x += dpp<XOR8>(x);
    return x;
}
__device__ __forceinline__ float wave_sum(float x) { return xrow16_sum(row16_sum(x)); }

#define STAMP(i) do {} while (0)

__device__ __forceinline__ void transpose8(v4u* r) {
    v4u o[8];
#pragma unroll
    for (int t = 0; t < 4; ++t)
#pragma unroll
        for (int jp = 0; jp < 4; ++jp) {
            const unsigned x0 = r[2 * jp][t], x1 = r[2 * jp + 1][t];
            o[2 * t][jp] = __builtin_amdgcn_perm(x1, x0, 0x05040100u);
            o[2 * t + 1][jp] = __builtin_amdgcn_perm(x1, x0, 0x07060302u);
        }
#pragma unroll
    for (int e = 0; e < 8; ++e) { asm volatile("" : "+v"(o[e])); r[e] = o[e]; }
}

__device__ __forceinline__ void lds_wait_ge_fast(LAS unsigned* p, unsigned v) {
#pragma nounroll
    for (unsigned spins = 0; spins < 16u * kSpinCap; ++spins) {
        if (lds_get(p) >= v) break;
    }
    CBAR();
}
template <int K, class LD>
__device__ __forceinline__ void stream_head(LD& ld, v4u (&r)[32], LAS unsigned* prev) {
    if constexpr (K == 0) lds_wait_ge(prev, NCH + K - WCH > 0 ? NCH + K - WCH : 1);
    lds_wait_ge_fast(prev, NCH + K - WCH + 1);
#pragma unroll
    for (int i = 0; i < CH; ++i) r[K * CH + i] = ld(K * CH + i);
    __builtin_amdgcn_sched_barrier(0);
    if constexpr (K + 1 < WCH) stream_head<K + 1>(ld, r, prev);
}
template <int K, class LD, class ON>
__device__ __forceinline__ void stream_step(LD& ld, ON& on, v4u (&r)[32], LAS unsigned* me) {
    if constexpr (K < NCH) {
        waitvm<(WCH - 1) * CH>();
        lds_set(me, K - WCH + 1);
#pragma unroll
        for (int i = 0; i < CH; ++i) r[K * CH + i] = ld(K * CH + i);
        __builtin_amdgcn_sched_barrier(0);
        on.template run<K - WCH>(r);
        __builtin_amdgcn_sched_barrier(0);
    } else {
        waitvm<(NCH + WCH - 1 - K) * CH>();
        lds_set(me, K - WCH + 1);
        on.template run<K - WCH>(r);
        __builtin_amdgcn_sched_barrier(0);
    }
    if constexpr (K + 1 < NCH + WCH) stream_step<K + 1>(ld, on, r, me);
}

template <int N>
__device__ __forceinline__ void gather(__amdgpu_buffer_rsrc_t rs, const int (&voff)[N], unsigned tag, v4u (&g)[N]) {
    unsigned pend = (1u << N) - 1u;
#pragma nounroll
    for (unsigned spins = 0; spins < kSpinCap; ++spins) {
        CBAR();
#pragma unroll
        for (int i = 0; i < N; ++i)
            if (pend & (1u << i)) g[i] = __builtin_amdgcn_raw_buffer_load_b128(rs, voff[i], 0, AUX_SC1);
#pragma unroll
        for (int i = 0; i < N; ++i)
            if ((pend & (1u << i)) && __all(g[i].y == tag && g[i].w == tag)) pend &= ~(1u << i);
        if (pend == 0u) break;
    }
    CBAR();
}

struct GemmOn {
    LAS unsigned* flag; unsigned need;
    const LAS unsigned char* pa0; const LAS unsigned char* pa1; int step;
    f32x4 (&acc)[8];
    v4u (&a)[4];
    int done;
    __device__ __forceinline__ void load_a() {
        a[0] = *(const LAS v4u*)(pa0); a[1] = *(const LAS v4u*)(pa1);
        a[2] = *(const LAS v4u*)(pa0 + step); a[3] = *(const LAS v4u*)(pa1 + step);
    }
    template <int G> __device__ __forceinline__ void mul(v4u (&r)[32]) {
#pragma unroll
        for (int e = 0; e < 8; ++e) acc[e] = mfma(a[G], r[8 * G + e], acc[e]);
    }
    template <int C> __device__ __forceinline__ void run(v4u (&r)[32]) {
        if constexpr (C & 1) {
            constexpr int G = C >> 1;
            transpose8(&r[8 * G]);
            if (done < 0 && lds_get(flag) >= need) {
                CBAR();
                load_a();
                done = 0;
                if constexpr (G >= 1) mul<0>(r);
                if constexpr (G >= 2) mul<1>(r);
                if constexpr (G >= 3) mul<2>(r);
            }
            if (done >= 0) mul<G>(r);
        }
    }
    __device__ __forceinline__ void finish(v4u (&r)[32]) {
        if (done < 0) {
            lds_wait_ge(flag, need);
            load_a();
            mul<0>(r); mul<1>(r); mul<2>(r); mul<3>(r);
        }
    }
};
struct KvOn {
    LAS unsigned char* ks; int lane;
    template <int C> __device__ __forceinline__ void run(v4u (&r)[32]) {
        if constexpr (C == 1 || C == 3) {
            constexpr int H = C >> 1;
            const int g = lane >> 4, c = lane & 15;
#pragma unroll
            for (int i = 0; i < 8; ++i) {
                const int key = 32 * H + 4 * i + g;
                *(LAS v4u*)(ks + key * 256 + ((c ^ ((4 * i + g) & 15)) * 16)) = r[8 * H + i];
            }
            CBAR();
#pragma unroll
            for (int t = 0; t < 2; ++t)
#pragma unroll
                for (int s = 0; s < 4; ++s) r[8 * H + 4 * t + s] = *(const LAS v4u*)(ks + (32 * H + 16 * t + c) * 256 + (((4 * s + g) ^ c) * 16));
        }
        if constexpr (C == 5) transpose8(&r[16]);
        if constexpr (C == 7) transpose8(&r[24]);
    }
};

__device__ __forceinline__ void fold_halves(const f32x4 (&acc)[8], int lane, float (&res)[8]) {
    const bool lo = (lane & 8) == 0;
#pragma unroll
    for (int e = 0; e < 8; ++e) {
        auto s = __builtin_amdgcn_permlane32_swap(f2u(acc[e][0]), f2u(acc[e][1]), false, false);
        auto t = __builtin_amdgcn_permlane32_swap(f2u(acc[e][2]), f2u(acc[e][3]), false, false);
        float s1 = u2f(s[0]) + dpp<XOR8>(u2f(s[1]));
        float s2 = u2f(t[1]) + dpp<XOR8>(u2f(t[0]));
        asm volatile("" : "+v"(s1), "+v"(s2));
        res[e] = lo ? s1 : s2;
    }
}

__device__ __forceinline__ void ln_rows(const v4u (&xv)[4], LAS unsigned char* dst, int lane) {
    float v[2][16], mean[2], rstd[2];
#pragma unroll
    for (int k = 0; k < 2; ++k) {
#pragma unroll
        for (int q = 0; q < 4; ++q) {
            v[k][2 * q] = bf_lo(xv[2 * k][q]); v[k][2 * q + 1] = bf_hi(xv[2 * k][q]);
            v[k][8 + 2 * q] = bf_lo(xv[2 * k + 1][q]); v[k][8 + 2 * q + 1] = bf_hi(xv[2 * k + 1][q]);
        }
        float s4[4];
#pragma unroll
        for (int q = 0; q < 4; ++q) s4[q] = (v[k][4 * q] + v[k][4 * q + 1]) + (v[k][4 * q + 2] + v[k][4 * q + 3]);
        mean[k] = (s4[0] + s4[1]) + (s4[2] + s4[3]);
    }
    mean[0] = wave_sum(mean[0]) * (1.0f / D);
    mean[1] = wave_sum(mean[1]) * (1.0f / D);
#pragma unroll
    for (int k = 0; k < 2; ++k) {
#pragma unroll
        for (int i = 0; i < 16; ++i) v[k][i] -= mean[k];
        float s4[4];
#pragma unroll
        for (int q = 0; q < 4; ++q)
            s4[q] = fmaf(v[k][4 * q], v[k][4 * q], v[k][4 * q + 1] * v[k][4 * q + 1]) + fmaf(v[k][4 * q + 2], v[k][4 * q + 2], v[k][4 * q + 3] * v[k][4 * q + 3]);
        rstd[k] = (s4[0] + s4[1]) + (s4[2] + s4[3]);
    }
    rstd[0] = wave_sum(rstd[0]);
    rstd[1] = wave_sum(rstd[1]);
#pragma unroll
    for (int k = 0; k < 2; ++k) {
        const float rs_ = 1.0f / sqrtf(rstd[k] * (1.0f / D) + 1e-5f);
#pragma unroll
        for (int h = 0; h < 2; ++h) {
            const v4u o = {pk_bf16(v[k][h * 8] * rs_, v[k][h * 8 + 1] * rs_), pk_bf16(v[k][h * 8 + 2] * rs_, v[k][h * 8 + 3] * rs_),
                           pk_bf16(v[k][h * 8 + 4] * rs_, v[k][h * 8 + 5] * rs_), pk_bf16(v[k][h * 8 + 6] * rs_, v[k][h * 8 + 7] * rs_)};
            *(LAS v4u*)(dst + k * XN_STR + h * 1024 + lane * 16) = o;
        }
    }
}
__device__ __forceinline__ void load_x_rows(const u16* x, int row0, int lane, v4u (&xv)[4]) {
    const GAS unsigned char* xp = (const GAS unsigned char*)x + row0 * 2048 + lane * 16;
#pragma unroll
    for (int i = 0; i < 4; ++i) xv[i] = *(const GAS v4u*)(xp + i * 1024);
}

__device__ __forceinline__ float gelu_tanh(float s) {
    const float u = 0.7978845608028654f * (s + 0.044715f * s * s * s);
    const float e = __expf(2.0f * u);
    const float th = 1.0f - 2.0f / (e + 1.0f);
    return 0.5f * s * (1.0f + th);
}
__device__ __forceinline__ float silu(float y) { return y / (1.0f + __expf(-y)); }

__global__ void __launch_bounds__(512, 2) fused_kernel(Params p) {
    __shared__ __attribute__((aligned(16))) unsigned char smem[LDS_BYTES];
    LAS unsigned char* L = (LAS unsigned char*)smem;
    LAS unsigned* FL = (LAS unsigned*)(L + FL_OFF);
    const int tid = threadIdx.x, lane = tid & 63, w = __builtin_amdgcn_readfirstlane(tid >> 6), bid = blockIdx.x;
    const int g = lane >> 4, c = lane & 15;
    const __amdgpu_buffer_rsrc_t rs = __builtin_amdgcn_make_buffer_rsrc((void*)p.ws, 0, WS_BYTES, 0x00020000);
    const unsigned long long tstart_ = __builtin_amdgcn_s_memrealtime();
    STAMP(0);
    constexpr float C2 = 0.08838834764831845f * 1.4426950408889634f;

    const int xcd = bid & 7, idx = bid >> 3;
    const bool has_w1 = bid < 224;
    const bool has_w2 = idx < 28;
    const int ks_o = xcd * 8 + (idx >> 2), nsl_o = idx & 3;
    const int t2 = xcd * 28 + idx, ks_2 = t2 >> 2, nsl_2 = t2 & 3;
    int prev;
    if (MK_ORDER == 0) prev = (w == 4 && !has_w1) ? 2 : w - 1;
    else prev = w == 3 ? 0 : w == 1 ? (has_w1 ? 3 : 0) : w == 5 ? 2 : w == 4 ? (has_w2 ? 5 : 2) : w - 1;

    const bool is_gemm = (w == 0 || w == 3 || w == 4 || w == 5);
    if (is_gemm) {
        v4u r[32];
        const bool ta = (w == 0 || w == 3);
        const u16* W = w == 0 ? p.wq : w == 3 ? p.w1 : w == 4 ? p.wo : p.w2;
        const int ldw = w == 0 ? NQ * 2 : w == 3 ? F * 2 : D * 2;
        const int nbytes = w == 3 || w == 5 ? D * F * 2 : D * NQ * 2;
        const int ks = ta ? (bid & 3) : w == 4 ? ks_o : ks_2;
        const int ns = ta ? (bid >> 2) : w == 4 ? nsl_o : nsl_2;
        const __amdgpu_buffer_rsrc_t rw = __builtin_amdgcn_make_buffer_rsrc((void*)W, 0, nbytes, 0x00020000);
        const int voffw = ta ? (ks * 256 + 32 * (c >> 3) + 8 * g) * ldw + (ns * 64 + 8 * (c & 7)) * 2
                             : (ks * 64 + 8 * g) * ldw + (ns * 256 + 8 * c) * 2;
        const int go1 = ta ? 64 * ldw : 256, go2 = ta ? 128 * ldw : 32 * ldw;
        auto ldg = [&](int i) {
            return __builtin_amdgcn_raw_buffer_load_b128(rw, voffw, (i & 7) * ldw + ((i >> 3) & 1) * go1 + (i >> 4) * go2, AUX_NT);
        };
        if (w == 0) {
            FL[lane] = 0u;
            if (lane < 32) ((LAS unsigned*)(L + ZR_OFF))[lane] = 0u;
#pragma unroll
            for (int i = 0; i < WCH * CH; ++i) r[i] = ldg(i);
            __builtin_amdgcn_sched_barrier(0);
            __syncthreads();
        } else {
            v4u xv[4];
            if (w >= 4) load_x_rows(p.x, (w - 4) * 2, lane, xv);
            __syncthreads();
            if (w >= 4) { ln_rows(xv, L + XN_OFF + (w - 4) * 2 * XN_STR, lane); lds_inc(FL + FL_XN, lane); STAMP(8); }
        }
        const bool has = w == 3 ? has_w1 : w == 5 ? has_w2 : true;
        LAS unsigned* trig = FL + (w == 0 ? FL_QT : w == 3 ? FL_HT : w == 4 ? FL_ST : FL_YT);
        if (has) {
            unsigned bbias = 0;
            if (w != 0) stream_head<0>(ldg, r, FL + FL_PROG + prev);
            f32x4 acc[8];
#pragma unroll
            for (int e = 0; e < 8; ++e) acc[e] = f32x4{0.f, 0.f, 0.f, 0.f};
            v4u a[4];
            const LAS unsigned char* pa0; const LAS unsigned char* pa1; int step;
            if (ta) {
                pa0 = L + XN_OFF + ks * 512 + (c & 7) * XN_STR + (32 * (c >> 3) + 8 * g) * 2; pa1 = pa0 + 128; step = 256;
            } else {
                const LAS unsigned char* act = L + (w == 4 ? OA_OFF : HA_OFF) + (c & 7) * AC_STR + g * 16;
                const LAS unsigned char* zr = L + ZR_OFF + g * 16;
                pa0 = c < 8 ? act : zr; pa1 = c < 8 ? zr : act; step = 64;
            }
            GemmOn on{FL + (ta ? FL_XN : w == 4 ? FL_O : FL_H), ta ? 4u : w == 4 ? 2u : 1u, pa0, pa1, step, acc, a, -1};
            stream_step<WCH>(ldg, on, r, FL + FL_PROG + w);
            STAMP(2);
            on.finish(r);
            STAMP(3);
            lds_wait_ge(FL + FL_T, 1);
            const unsigned tag = __builtin_amdgcn_readfirstlane(FL[FL_TV]);
            __builtin_amdgcn_s_setprio(3);
            if (ta) {
                float res[8];
                fold_halves(acc, lane, res);
                const int b = 4 * (g & 1) + (g >> 1) + 2 * (c >> 3);
                const int o = (w == 0 ? QP_OFF + (ks * 8 + b) * QP_ROW : HP_OFF + (ks * 8 + b) * HP_ROW) + ns * 256 + (c & 7) * 16;
#pragma unroll
                for (int u = 0; u < 2; ++u)
                    __builtin_amdgcn_raw_buffer_store_b128(v4u{pk_bf16(res[4 * u], res[4 * u + 1]), tag, pk_bf16(res[4 * u + 2], res[4 * u + 3]), tag}, rs, o, u * 128, AUX_SC1);
            } else {
                const int o = (w == 4 ? SP_OFF + (ns * 64 + ks) * 8192 : YP_OFF + (ns * 56 + ks) * 8192) + lane * 16;
#pragma unroll
                for (int rr = 0; rr < 4; ++rr)
#pragma unroll
                    for (int eh = 0; eh < 2; ++eh)
                        __builtin_amdgcn_raw_buffer_store_b128(
                            v4u{pk_bf16(acc[4 * eh][rr], acc[4 * eh + 1][rr]), tag, pk_bf16(acc[4 * eh + 2][rr], acc[4 * eh + 3][rr]), tag}, rs, o,
                            (2 * rr + eh) * 1024, AUX_SC1);
            }
            lds_set(trig, 1);
            STAMP(4);
            (void)bbias;
        } else {
            if (w == 3) lds_wait_ge(FL + FL_PROG + (MK_ORDER == 0 ? 4 : 1), NCH / 2);
            else lds_wait_ge(FL + FL_PROG + 4, NCH);
            lds_set(trig, 1);
        }
        if (w == 3 && has_w2) {
            lds_wait_ge(FL + FL_T, 1);
            const unsigned tag = __builtin_amdgcn_readfirstlane(FL[FL_TV]);
            const int n4 = 8 * (c & 7) + 4 * (c >> 3);
            const v2u bb = *(const GAS v2u*)(p.b1 + ks_2 * 64 + n4);
            int vo[8];
#pragma unroll
            for (int i = 0; i < 8; ++i) vo[i] = HP_OFF + ((i >> 1) * 8 + (i & 1) * 4 + g) * HP_ROW + ks_2 * 256 + c * 16;
            __builtin_amdgcn_s_sleep(MK_DH);
            v4u h8[8];
            gather<8>(rs, vo, tag, h8);
            STAMP(5);
#pragma unroll
            for (int bh = 0; bh < 2; ++bh) {
                float v0 = (bf_lo(h8[bh].x) + bf_lo(h8[2 + bh].x)) + (bf_lo(h8[4 + bh].x) + bf_lo(h8[6 + bh].x)) + bf_lo(bb.x);
                float v1 = (bf_hi(h8[bh].x) + bf_hi(h8[2 + bh].x)) + (bf_hi(h8[4 + bh].x) + bf_hi(h8[6 + bh].x)) + bf_hi(bb.x);
                float v2 = (bf_lo(h8[bh].z) + bf_lo(h8[2 + bh].z)) + (bf_lo(h8[4 + bh].z) + bf_lo(h8[6 + bh].z)) + bf_lo(bb.y);
                float v3 = (bf_hi(h8[bh].z) + bf_hi(h8[2 + bh].z)) + (bf_hi(h8[4 + bh].z) + bf_hi(h8[6 + bh].z)) + bf_hi(bb.y);
                *(LAS v2u*)(L + HA_OFF + (bh * 4 + g) * AC_STR + n4 * 2) = v2u{pk_bf16(gelu_tanh(v0), gelu_tanh(v1)), pk_bf16(gelu_tanh(v2), gelu_tanh(v3))};
            }
            lds_set(FL + FL_H, 1);
            STAMP(6);
        }
    } else if (w == 1 || w == 2) {
        const int gid = xcd * 4 + (idx >> 3), sp = idx & 7;
        const size_t kvbase = ((size_t)gid * SK + (SK - WIN) + sp * 128 + (w - 1) * 64) * (DH * 2);
        const __amdgpu_buffer_rsrc_t rk = __builtin_amdgcn_make_buffer_rsrc((void*)((const unsigned char*)p.kc + kvbase), 0, 64 * 256, 0x00020000);
        const __amdgpu_buffer_rsrc_t rv = __builtin_amdgcn_make_buffer_rsrc((void*)((const unsigned char*)p.vc + kvbase), 0, 64 * 256, 0x00020000);
        const int vvo = g * 1024 + c * 16;
        auto ld = [&](int i) {
            if (i < 16) return __builtin_amdgcn_raw_buffer_load_b128(rk, lane * 16, i * 1024, AUX_NT);
            const int j = i - 16;
            return __builtin_amdgcn_raw_buffer_load_b128(rv, vvo, (j >> 3) * 8192 + ((j >> 2) & 1) * 4096 + (j & 3) * 256, AUX_NT);
        };
        KvOn on{L + KS_OFF + (w - 1) * 16384, lane};
        v4u r[32];
        __syncthreads();
        stream_head<0>(ld, r, FL + FL_PROG + prev);
        stream_step<WCH>(ld, on, r, FL + FL_PROG + w);
        STAMP(2);
        lds_wait_ge(FL + FL_Q, 2);
        STAMP(3);
        __builtin_amdgcn_s_setprio(3);
        v4u qf[4];
#pragma unroll
        for (int s = 0; s < 4; ++s) qf[s] = *(const LAS v4u*)(L + QS_OFF + (c & 7) * QS_STR + (32 * s + 8 * g) * 2);
        f32x4 S[4];
#pragma unroll
        for (int t = 0; t < 4; ++t) S[t] = f32x4{0.f, 0.f, 0.f, 0.f};
#pragma unroll
        for (int s = 0; s < 4; ++s)
#pragma unroll
            for (int t = 0; t < 4; ++t) S[t] = mfma(r[4 * t + s], qf[s], S[t]);
        float mx = fmaxf(fmaxf(fmaxf(S[0][0], S[0][1]), fmaxf(S[0][2], S[0][3])), fmaxf(fmaxf(S[1][0], S[1][1]), fmaxf(S[1][2], S[1][3])));
        mx = fmaxf(mx, fmaxf(fmaxf(fmaxf(S[2][0], S[2][1]), fmaxf(S[2][2], S[2][3])), fmaxf(fmaxf(S[3][0], S[3][1]), fmaxf(S[3][2], S[3][3]))));
        mx = xrow16_max(mx);
        LAS float* st1 = (LAS float*)(L + ST1_OFF);
        f32x4 O[4];
        float lsum = 0.f, m = mx;
        if (w == 2) {
            lds_wait_ge(FL + FL_KV1, 1);
            const float m1 = st1[c & 7], l1 = st1[8 + (c & 7)];
            m = fmaxf(mx, m1);
            const float f1 = __builtin_amdgcn_exp2f((m1 - m) * C2);
            lsum = (g == 0) ? f1 * l1 : 0.f;
            if (lane < 8) st1[16 + lane] = f1;
            CBAR();
            const f32x4 f4 = *(const LAS f32x4*)(st1 + 16 + 4 * (g & 1));
#pragma unroll
            for (int e = 0; e < 4; ++e) O[e] = *(const LAS f32x4*)(L + KV_OFF + (e * 64 + lane) * 16) * f4;
        } else {
#pragma unroll
            for (int e = 0; e < 4; ++e) O[e] = f32x4{0.f, 0.f, 0.f, 0.f};
        }
        const float mc = m * C2;
        float ls4[4];
#pragma unroll
        for (int t = 0; t < 4; ++t) {
#pragma unroll
            for (int q = 0; q < 4; ++q) S[t][q] = __builtin_amdgcn_exp2f(fmaf(S[t][q], C2, -mc));
            ls4[t] = (S[t][0] + S[t][1]) + (S[t][2] + S[t][3]);
        }
        lsum += (ls4[0] + ls4[1]) + (ls4[2] + ls4[3]);
        const bool lo8 = c < 8;
#pragma unroll
        for (int u = 0; u < 2; ++u) {
            const v4u pa = {pk_bf16(S[2 * u][0], S[2 * u][1]), pk_bf16(S[2 * u][2], S[2 * u][3]),
                            pk_bf16(S[2 * u + 1][0], S[2 * u + 1][1]), pk_bf16(S[2 * u + 1][2], S[2 * u + 1][3])};
            const v4u z = {0u, 0u, 0u, 0u};
            const v4u p0 = lo8 ? pa : z, p1 = lo8 ? z : pa;
#pragma unroll
            for (int e = 0; e < 4; ++e) O[e] = mfma(p0, r[16 + 8 * u + e], O[e]);
#pragma unroll
            for (int e = 0; e < 4; ++e) O[e] = mfma(p1, r[16 + 8 * u + 4 + e], O[e]);
        }
        lsum = xrow16_sum(lsum);
        if (w == 1) {
#pragma unroll
            for (int e = 0; e < 4; ++e) *(LAS f32x4*)(L + KV_OFF + (e * 64 + lane) * 16) = O[e];
            if (lane < 8) { st1[lane] = m; st1[8 + lane] = lsum; }
            lds_set(FL + FL_KV1, 1);
            STAMP(4);
        } else {
            const unsigned tag = __builtin_amdgcn_readfirstlane(FL[FL_TV]);
            const int obase = OP_OFF + ((gid * 8 + sp) * 8 + 4 * (g & 1)) * 512 + (g >> 1) * 256 + c * 16;
#pragma unroll
            for (int rr = 0; rr < 4; ++rr)
                __builtin_amdgcn_raw_buffer_store_b128(v4u{pk_bf16(O[0][rr], O[1][rr]), tag, pk_bf16(O[2][rr], O[3][rr]), tag}, rs, obase, rr * 512, AUX_SC1);
            if (lane < 8) __builtin_amdgcn_raw_buffer_store_b128(v4u{f2u(m), tag, f2u(lsum), tag}, rs, OM_OFF + ((gid * 8 + lane) * 8 + sp) * 16, 0, AUX_SC1);
            lds_set(FL + FL_OT, 1);
            STAMP(4);
        }
    } else {
        const int hw = w - 6;
        unsigned ep = 0;
        if (hw == 1) ep = __hip_atomic_load((GAS unsigned*)(p.ws + WS_EPOCH), __ATOMIC_RELAXED, __HIP_MEMORY_SCOPE_AGENT);
        unsigned touch = 0;
        {
            v4u xv[4];
            load_x_rows(p.x, 4 + hw * 2, lane, xv);
            __syncthreads();
            if (hw == 1) {
                FL[FL_TV] = __builtin_amdgcn_readfirstlane(ep) + 1u;
                lds_set(FL + FL_T, 1);
            }
            ln_rows(xv, L + XN_OFF + (4 + hw * 2) * XN_STR, lane);
            lds_inc(FL + FL_XN, lane);
            STAMP(8);
            asm volatile("" :: "v"(touch));
        }
        __builtin_amdgcn_s_setprio(3);
        lds_wait_ge(FL + FL_T, 1);
        const unsigned tag = __builtin_amdgcn_readfirstlane(FL[FL_TV]);
        {
            const int gid = xcd * 4 + (idx >> 3), bq = gid >> 2, hk = gid & 3;
            int vo[8];
#pragma unroll
            for (int i = 0; i < 8; ++i) vo[i] = QP_OFF + ((i >> 1) * 8 + bq) * QP_ROW + (hk * 16 + hw * 8 + (i & 1) * 4 + g) * 256 + c * 16;
            lds_wait_ge(FL + FL_QT, 1);
            __builtin_amdgcn_s_sleep(MK_DQ);
            v4u q8[8];
            gather<8>(rs, vo, tag, q8);
            STAMP(3);
            const int n4 = 8 * (c & 7) + 4 * (c >> 3);
#pragma unroll
            for (int sh = 0; sh < 2; ++sh) {
                const float v0 = (bf_lo(q8[sh].x) + bf_lo(q8[2 + sh].x)) + (bf_lo(q8[4 + sh].x) + bf_lo(q8[6 + sh].x));
                const float v1 = (bf_hi(q8[sh].x) + bf_hi(q8[2 + sh].x)) + (bf_hi(q8[4 + sh].x) + bf_hi(q8[6 + sh].x));
                const float v2 = (bf_lo(q8[sh].z) + bf_lo(q8[2 + sh].z)) + (bf_lo(q8[4 + sh].z) + bf_lo(q8[6 + sh].z));
                const float v3 = (bf_hi(q8[sh].z) + bf_hi(q8[2 + sh].z)) + (bf_hi(q8[4 + sh].z) + bf_hi(q8[6 + sh].z));
                const int n = (hw * 8 + sh * 4 + g) * 64 + n4;
                *(LAS v2u*)(L + QS_OFF + (n >> 7) * QS_STR + (n & 127) * 2) = v2u{pk_bf16(v0, v1), pk_bf16(v2, v3)};
            }
            lds_inc(FL + FL_Q, lane);
            STAMP(4);
        }
        {
            const int head = ks_o >> 1, dh = ks_o & 1, hk = head >> 3, hh = head & 7;
            const int gidb = (4 * hw + g) * 4 + hk;
            int vo[9];
#pragma unroll
            for (int i = 0; i < 8; ++i) vo[i] = OP_OFF + ((gidb * 8 + i) * 8 + hh) * 512 + (c >> 3) * 256 + (8 * dh + (c & 7)) * 16;
            vo[8] = OM_OFF + ((gidb * 8 + hh) * 8 + (c & 7)) * 16;
            lds_wait_ge(FL + FL_OT, 1);
            __builtin_amdgcn_s_sleep(MK_DO);
            v4u o9[9];
            gather<9>(rs, vo, tag, o9);
            STAMP(5);
            const float ms = u2f(o9[8].x), ls = u2f(o9[8].z);
            float M = ms;
            M = fmaxf(M, dpp<XOR1>(M)); M = fmaxf(M, dpp<XOR2>(M)); M = fmaxf(M, dpp<XOR7>(M));
            const float we = __builtin_amdgcn_exp2f((ms - M) * C2);
            float Ls = we * ls;
            Ls += dpp<XOR1>(Ls); Ls += dpp<XOR2>(Ls); Ls += dpp<XOR7>(Ls);
            const float wn = we / Ls;
            LAS float* wl = (LAS float*)(L + ST1_OFF + 128) + hw * 32 + g * 8;
            if (c < 8) wl[c] = wn;
            CBAR();
            const f32x4 wa = *(const LAS f32x4*)wl, wb = *(const LAS f32x4*)(wl + 4);
            float v0 = 0.f, v1 = 0.f, v2 = 0.f, v3 = 0.f;
#pragma unroll
            for (int i = 0; i < 8; ++i) {
                const float wi = i < 4 ? wa[i & 3] : wb[i & 3];
                v0 = fmaf(wi, bf_lo(o9[i].x), v0); v1 = fmaf(wi, bf_hi(o9[i].x), v1);
                v2 = fmaf(wi, bf_lo(o9[i].z), v2); v3 = fmaf(wi, bf_hi(o9[i].z), v3);
            }
            *(LAS v2u*)(L + OA_OFF + (4 * hw + g) * AC_STR + (8 * (c & 7) + 4 * (c >> 3)) * 2) = v2u{pk_bf16(v0, v1), pk_bf16(v2, v3)};
            lds_inc(FL + FL_O, lane);
            STAMP(6);
        }
        if (hw == 1) {
            const int nsl = bid & 3, j = bid >> 2, st = j >> 3, G = (j & 7) >> 1, lam = lane & 7, cc = 8 * (j & 1) + lam, src = lane >> 3;
            const int b = 4 * (G & 1) + (st >> 1), n = 256 * nsl + 128 * (G >> 1) + 8 * cc + 4 * (st & 1);
            const v2u xx = *(const GAS v2u*)(p.x + b * D + n), bb = *(const GAS v2u*)(p.b2 + n);
            const int uo = (st * 64 + 8 * (j & 7) + lam) * 16;
            float sv[4], yv[4];
            {
                int vo[8];
#pragma unroll
                for (int i = 0; i < 8; ++i) vo[i] = SP_OFF + (nsl * 64 + 8 * i + src) * 8192 + uo;
                lds_wait_ge(FL + FL_ST, 1);
                __builtin_amdgcn_s_sleep(MK_DF);
                v4u f8[8];
                gather<8>(rs, vo, tag, f8);
                sv[0] = sv[1] = sv[2] = sv[3] = 0.f;
#pragma unroll
                for (int i = 0; i < 8; ++i) { sv[0] += bf_lo(f8[i].x); sv[1] += bf_hi(f8[i].x); sv[2] += bf_lo(f8[i].z); sv[3] += bf_hi(f8[i].z); }
#pragma unroll
                for (int q = 0; q < 4; ++q) { sv[q] += dpp<XOR8>(sv[q]); sv[q] = xrow16_sum(sv[q]); }
            }
            STAMP(7);
            {
                int vo[7];
#pragma unroll
                for (int i = 0; i < 7; ++i) vo[i] = YP_OFF + (nsl * 56 + 8 * i + src) * 8192 + uo;
                lds_wait_ge(FL + FL_YT, 1);
                __builtin_amdgcn_s_sleep(MK_DF);
                v4u f7[7];
                gather<7>(rs, vo, tag, f7);
                yv[0] = yv[1] = yv[2] = yv[3] = 0.f;
#pragma unroll
                for (int i = 0; i < 7; ++i) { yv[0] += bf_lo(f7[i].x); yv[1] += bf_hi(f7[i].x); yv[2] += bf_lo(f7[i].z); yv[3] += bf_hi(f7[i].z); }
#pragma unroll
                for (int q = 0; q < 4; ++q) { yv[q] += dpp<XOR8>(yv[q]); yv[q] = xrow16_sum(yv[q]); }
            }
            const float o0 = bf_lo(xx.x) + sv[0] * silu(yv[0] + bf_lo(bb.x)), o1 = bf_hi(xx.x) + sv[1] * silu(yv[1] + bf_hi(bb.x));
            const float o2 = bf_lo(xx.y) + sv[2] * silu(yv[2] + bf_lo(bb.y)), o3 = bf_hi(xx.y) + sv[3] * silu(yv[3] + bf_hi(bb.y));
            if (lane < 8) *(GAS v2u*)(p.out + b * D + n) = v2u{pk_bf16(o0, o1), pk_bf16(o2, o3)};
            if (bid == 0 && lane == 0) __hip_atomic_store((GAS unsigned*)(p.ws + WS_EPOCH), tag, __ATOMIC_RELAXED, __HIP_MEMORY_SCOPE_AGENT);
            STAMP(1);
        }
    }
    if (w == 7) { while (__builtin_amdgcn_s_memrealtime() - tstart_ < MK_FIXDUR) __builtin_amdgcn_s_sleep(1); }
}

}

extern "C" void kernel_launch(void* const* d_in, const int* in_sizes, int n_in, void* d_out, int out_size, void* d_ws, size_t ws_size,
                              hipStream_t stream) {
    mk::Params p;
    p.x = (const u16*)d_in[0];
    p.kc = (const u16*)d_in[1];
    p.vc = (const u16*)d_in[2];
    p.wq = (const u16*)d_in[3];
    p.wo = (const u16*)d_in[4];
    p.w1 = (const u16*)d_in[6];
    p.b1 = (const u16*)d_in[7];
    p.w2 = (const u16*)d_in[8];
    p.b2 = (const u16*)d_in[9];
    p.out = (u16*)d_out;
    p.ws = (unsigned char*)d_ws;
    mk::fused_kernel<<<256, 512, 0, stream>>>(p);
}
```

```cpp
#include <hip/hip_runtime.h>
#include <hip/hip_bf16.h>
#include <stdint.h>

#define GAS __attribute__((address_space(1)))
#define LAS __attribute__((address_space(3)))
typedef unsigned short u16;
typedef unsigned v4u __attribute__((ext_vector_type(4)));
typedef unsigned v2u __attribute__((ext_vector_type(2)));
typedef short v4s __attribute__((ext_vector_type(4)));
typedef float f32x4 __attribute__((ext_vector_type(4)));
typedef float f32x2 __attribute__((ext_vector_type(2)));
typedef __bf16 bf16x8 __attribute__((ext_vector_type(8)));
typedef __bf16 bf16x2 __attribute__((ext_vector_type(2)));

#define MK_DQ 55
#define MK_DO 55
#define MK_DH 55
#define MK_DS 45
#define MK_DY 45
#define MK_STAMPS 0
#define MK_WIN 24
#define MK_BACKOFF 0

namespace mk {

constexpr int B = 8, D = 1024, SK = 4096, DH = 128, WIN = 1024, F = 3584, NQ = 4096;
constexpr int NWAVE = 9;

constexpr int WS_EPOCH = 0;
constexpr int QP_ROW = 16384, QP_OFF = 4096;
constexpr int HP_ROW = 14336, HP_OFF = QP_OFF + 32 * QP_ROW;
constexpr int OP_OFF = HP_OFF + 32 * HP_ROW;
constexpr int OM_OFF = OP_OFF + 32 * 8 * 8 * 512;
constexpr int SP_OFF = OM_OFF + 32 * 8 * 8 * 16;
constexpr int YP_OFF = SP_OFF + 4 * 64 * 8192;
constexpr int WS_END = YP_OFF + 4 * 56 * 8192;
constexpr int ST_OFF = WS_END;
constexpr int WS_BYTES = ST_OFF + 256 * NWAVE * 16 * 8;

constexpr int R0 = 0, R1 = 32768, R2 = 65536, R3 = 98304;
constexpr int XN_STR = 2048 + 16, XN_OFF = 131072;
constexpr int QS_STR = 256 + 16, QS_OFF = XN_OFF + 8 * XN_STR;
constexpr int AC_STR = 128 + 16, OA_OFF = QS_OFF + 8 * QS_STR;
constexpr int HA_OFF = OA_OFF + 8 * AC_STR;
constexpr int ML_OFF = HA_OFF + 8 * AC_STR;
constexpr int FL_OFF = ML_OFF + 1024;
constexpr int LDS_USED = FL_OFF + 256;
constexpr int LDS_BYTES = LDS_USED;
static_assert(LDS_BYTES <= 160 * 1024 && (ML_OFF % 16) == 0 && (OA_OFF % 16) == 0 && (QS_OFF % 16) == 0, "lds");
enum { FL_LANDED = 0, FL_R0 = 1, FL_R1 = 2, FL_XN = 3, FL_QT = 4, FL_Q = 5, FL_MRG = 6, FL_OT = 7, FL_O = 8, FL_ST = 9, FL_HT = 10, FL_H = 11, FL_YT = 12,
       FL_S = 13, FL_T = 14, FL_TV = 15, FL_R2 = 16 };

struct Params {
    const u16 *x, *kc, *vc, *wq, *wo, *w1, *b1, *w2, *b2;
    u16* out;
    unsigned char* ws;
};

constexpr unsigned kSpinCap = 1u << 17;
constexpr int AUX_NT = 2, AUX_SC1 = 16;

__device__ __forceinline__ float bf_lo(unsigned u) { return __builtin_bit_cast(float, u << 16); }
__device__ __forceinline__ float bf_hi(unsigned u) { return __builtin_bit_cast(float, u & 0xffff0000u); }
__device__ __forceinline__ unsigned pk_bf16(float lo, float hi) {
    f32x2 v = {lo, hi};
    return __builtin_bit_cast(unsigned, __builtin_convertvector(v, bf16x2));
}
__device__ __forceinline__ f32x4 mfma(v4u a, v4u b, f32x4 c) {
    return __builtin_amdgcn_mfma_f32_16x16x32_bf16(__builtin_bit_cast(bf16x8, a), __builtin_bit_cast(bf16x8, b), c, 0, 0, 0);
}
__device__ __forceinline__ unsigned f2u(float f) { return __builtin_bit_cast(unsigned, f); }
__device__ __forceinline__ float u2f(unsigned u) { return __builtin_bit_cast(float, u); }

#define CBAR() asm volatile("" ::: "memory")
template <int N> __device__ __forceinline__ void waitvm() { asm volatile("s_waitcnt vmcnt(%0)" ::"i"(N) : "memory"); }
__device__ __forceinline__ void waitlds() { asm volatile("s_waitcnt lgkmcnt(0)" ::: "memory"); }

__device__ __forceinline__ void lds_set(LAS unsigned* p, unsigned v) {
    CBAR();
    __hip_atomic_store(p, v, __ATOMIC_RELAXED, __HIP_MEMORY_SCOPE_WORKGROUP);
    CBAR();
}
__device__ __forceinline__ void lds_inc(LAS unsigned* p, int lane) {
    CBAR();
    if (lane == 0) __hip_atomic_fetch_add(p, 1u, __ATOMIC_RELAXED, __HIP_MEMORY_SCOPE_WORKGROUP);
    CBAR();
}
__device__ __forceinline__ unsigned lds_get(LAS unsigned* p) {
    return (unsigned)__builtin_amdgcn_readfirstlane(__hip_atomic_load(p, __ATOMIC_RELAXED, __HIP_MEMORY_SCOPE_WORKGROUP));
}
__device__ __forceinline__ void lds_wait_ge(LAS unsigned* p, unsigned v) {
#pragma nounroll
    for (unsigned spins = 0; spins < kSpinCap; ++spins) {
        if (lds_get(p) >= v) break;
        __builtin_amdgcn_s_sleep(1);
    }
    CBAR();
}

__device__ __forceinline__ void lds_wait_ge_fast(LAS unsigned* p, unsigned v) {
#pragma nounroll
    for (unsigned spins = 0; spins < 4u * kSpinCap; ++spins) {
        if (lds_get(p) >= v) break;
    }
    CBAR();
}
__device__ __forceinline__ void lds_set_asm(unsigned addr, unsigned v) {
    asm volatile("ds_write_b32 %0, %1" ::"v"(addr), "v"(v) : "memory");
}
__device__ __forceinline__ void lds_wait_ge_asm(unsigned addr, unsigned v) {
#pragma nounroll
    for (unsigned spins = 0; spins < kSpinCap; ++spins) {
        unsigned x;
        asm volatile("ds_read_b32 %0, %1\n\ts_waitcnt lgkmcnt(0)" : "=v"(x) : "v"(addr) : "memory");
        if ((unsigned)__builtin_amdgcn_readfirstlane(x) >= v) break;
        __builtin_amdgcn_s_sleep(1);
    }
}
__device__ __forceinline__ float xrow16_max(float x) {
    auto s = __builtin_amdgcn_permlane16_swap(f2u(x), f2u(x), false, false);
    x = fmaxf(u2f(s[0]), u2f(s[1]));
    auto t = __builtin_amdgcn_permlane32_swap(f2u(x), f2u(x), false, false);
    return fmaxf(u2f(t[0]), u2f(t[1]));
}
__device__ __forceinline__ float x16_sum(float x) {
    auto s = __builtin_amdgcn_permlane16_swap(f2u(x), f2u(x), false, false);
    return u2f(s[0]) + u2f(s[1]);
}
__device__ __forceinline__ float x32_sum(float x) {
    auto t = __builtin_amdgcn_permlane32_swap(f2u(x), f2u(x), false, false);
    return u2f(t[0]) + u2f(t[1]);
}
__device__ __forceinline__ float xrow16_sum(float x) { return x32_sum(x16_sum(x)); }
template <int CTRL> __device__ __forceinline__ float dpp(float x) {
    return u2f(__builtin_amdgcn_mov_dpp(f2u(x), CTRL, 0xf, 0xf, true));
}
constexpr int XOR1 = 0xB1, XOR2 = 0x4E, XOR7 = 0x141, XOR8 = 0x128;
__device__ __forceinline__ float row16_sum(float x) {
    x += dpp<XOR1>(x); x += dpp<XOR2>(x); x += dpp<XOR7>(x); x += dpp<XOR8>(x);
    return x;
}
__device__ __forceinline__ float wave_sum(float x) { return xrow16_sum(row16_sum(x)); }

#define STAMP(i) do {} while (0)

__device__ __forceinline__ f32x4 sel4(bool hi, f32x4 a_hi, f32x4 a_lo) {
    asm volatile("" : "+v"(a_hi), "+v"(a_lo));
    return f32x4{hi ? a_hi[0] : a_lo[0], hi ? a_hi[1] : a_lo[1], hi ? a_hi[2] : a_lo[2], hi ? a_hi[3] : a_lo[3]};
}
__device__ __forceinline__ v4u tr_frag(const LAS unsigned char* p0, const LAS unsigned char* p1) {
    const v4s a = __builtin_amdgcn_ds_read_tr16_b64_v4i16((LAS v4s*)p0);
    const v4s b = __builtin_amdgcn_ds_read_tr16_b64_v4i16((LAS v4s*)p1);
    const v2u x = __builtin_bit_cast(v2u, a), y = __builtin_bit_cast(v2u, b);
    return v4u{x.x, x.y, y.x, y.y};
}

template <int N>
__device__ __forceinline__ void gather(__amdgpu_buffer_rsrc_t rs, const int (&voff)[N], unsigned tag, v4u (&g)[N]) {
    unsigned pend = (1u << N) - 1u;
#pragma nounroll
    for (unsigned spins = 0; spins < kSpinCap; ++spins) {
        CBAR();
#pragma unroll
        for (int i = 0; i < N; ++i)
            if (pend & (1u << i)) g[i] = __builtin_amdgcn_raw_buffer_load_b128(rs, voff[i], 0, AUX_SC1);
#pragma unroll
        for (int i = 0; i < N; ++i)
            if ((pend & (1u << i)) && __all(g[i].y == tag && g[i].w == tag)) pend &= ~(1u << i);
        if (pend == 0u) break;
    }
    CBAR();
}

__device__ __forceinline__ void ln_row(const v4u (&xv)[2], LAS unsigned char* dst, int lane) {
    float v[16];
#pragma unroll
    for (int q = 0; q < 4; ++q) {
        v[2 * q] = bf_lo(xv[0][q]); v[2 * q + 1] = bf_hi(xv[0][q]);
        v[8 + 2 * q] = bf_lo(xv[1][q]); v[8 + 2 * q + 1] = bf_hi(xv[1][q]);
    }
    float s4[4], t4[4];
#pragma unroll
    for (int q = 0; q < 4; ++q) s4[q] = (v[4 * q] + v[4 * q + 1]) + (v[4 * q + 2] + v[4 * q + 3]);
    const float mean = wave_sum((s4[0] + s4[1]) + (s4[2] + s4[3])) * (1.0f / D);
#pragma unroll
    for (int i = 0; i < 16; ++i) v[i] -= mean;
#pragma unroll
    for (int q = 0; q < 4; ++q) t4[q] = fmaf(v[4 * q], v[4 * q], v[4 * q + 1] * v[4 * q + 1]) + fmaf(v[4 * q + 2], v[4 * q + 2], v[4 * q + 3] * v[4 * q + 3]);
    const float var = wave_sum((t4[0] + t4[1]) + (t4[2] + t4[3])) * (1.0f / D);
    const float rs_ = 1.0f / sqrtf(var + 1e-5f);
#pragma unroll
    for (int h = 0; h < 2; ++h) {
        const v4u o = {pk_bf16(v[h * 8] * rs_, v[h * 8 + 1] * rs_), pk_bf16(v[h * 8 + 2] * rs_, v[h * 8 + 3] * rs_),
                       pk_bf16(v[h * 8 + 4] * rs_, v[h * 8 + 5] * rs_), pk_bf16(v[h * 8 + 6] * rs_, v[h * 8 + 7] * rs_)};
        *(LAS v4u*)(dst + h * 1024 + lane * 16) = o;
    }
}

__device__ __forceinline__ float gelu_tanh(float s) {
    const float u = 0.7978845608028654f * (s + 0.044715f * s * s * s);
    const float e = __expf(2.0f * u);
    const float th = 1.0f - 2.0f / (e + 1.0f);
    return 0.5f * s * (1.0f + th);
}
__device__ __forceinline__ float silu(float y) { return y / (1.0f + __expf(-y)); }

__device__ __forceinline__ void gemm_a(LAS unsigned char* L, int R, LAS unsigned* FL, int need_landed, LAS unsigned* free_flag, int nt, int ks, int lane,
                                       __amdgpu_buffer_rsrc_t rs, int out_off, unsigned& tag) {
    const int g = lane >> 4, q = (lane & 15) >> 2, p = lane & 3, c = lane & 15;
    const LAS unsigned char* ap = L + R + (8 * g + q) * 128 + (((2 * nt + (p >> 1)) ^ (2 * ((q >> 1) & 1) + 4 * (g & 1))) * 16) + 8 * (p & 1);
    const LAS unsigned char* bp = L + XN_OFF + (c & 7) * XN_STR + (ks * 256 + 8 * g) * 2;
    lds_wait_ge(FL + FL_XN, 8);
    v4u bx[8];
#pragma unroll
    for (int s = 0; s < 8; ++s) bx[s] = *(const LAS v4u*)(bp + s * 64);
    lds_wait_ge(FL + FL_LANDED, need_landed);
    v4u a[8];
#pragma unroll
    for (int s = 0; s < 8; ++s) a[s] = tr_frag(ap + s * 4096, ap + s * 4096 + 512);
    f32x4 acc0 = {0.f, 0.f, 0.f, 0.f}, acc1 = {0.f, 0.f, 0.f, 0.f};
#pragma unroll
    for (int s = 0; s < 8; s += 2) { acc0 = mfma(a[s], bx[s], acc0); acc1 = mfma(a[s + 1], bx[s + 1], acc1); }
    acc0 += acc1;
    tag = __builtin_amdgcn_readfirstlane(FL[FL_TV]);
    if (c < 8) __builtin_amdgcn_raw_buffer_store_b128(v4u{pk_bf16(acc0[0], acc0[1]), tag, pk_bf16(acc0[2], acc0[3]), tag}, rs, out_off + (4 * nt + g) * 16, 0, AUX_SC1);
    if (free_flag) lds_inc(free_flag, lane);
}
__device__ __forceinline__ void gemm_b(LAS unsigned char* L, int R, LAS unsigned* FL, int need_landed, LAS unsigned* act_flag, int act_off, int xi, int lane,
                                       __amdgpu_buffer_rsrc_t rs, int out_off, unsigned tag) {
    const int g = lane >> 4, q = (lane & 15) >> 2, p = lane & 3, c = lane & 15;
    const LAS unsigned char* rowp = L + R + (8 * g + q) * 512 + (p >> 1) * 16 + 8 * (p & 1);
    lds_wait_ge(FL + FL_LANDED, need_landed);
    v4u a[4][2];
#pragma unroll
    for (int n = 0; n < 4; ++n) {
        const LAS unsigned char* ap = rowp + (((8 * xi + 2 * n) ^ (2 * q + 8 * (g & 1))) * 16);
#pragma unroll
        for (int s = 0; s < 2; ++s) a[n][s] = tr_frag(ap + s * 16384, ap + s * 16384 + 2048);
    }
    lds_wait_ge_fast(act_flag, 4);
    const LAS unsigned char* bp = L + act_off + (c & 7) * AC_STR + g * 16;
    const v4u b0 = *(const LAS v4u*)bp, b1 = *(const LAS v4u*)(bp + 64);
    f32x4 acc[4];
#pragma unroll
    for (int n = 0; n < 4; ++n) acc[n] = mfma(a[n][0], b0, f32x4{0.f, 0.f, 0.f, 0.f});
#pragma unroll
    for (int n = 0; n < 4; ++n) acc[n] = mfma(a[n][1], b1, acc[n]);
    const bool hi = c >= 8;
    const int o = out_off + (c & 7) * 1024 + (16 * xi + (hi ? 4 : 0) + g) * 16;
#pragma unroll
    for (int h = 0; h < 2; ++h) {
        const f32x4 v = sel4(hi, acc[2 * h + 1], acc[2 * h]);
        __builtin_amdgcn_raw_buffer_store_b128(v4u{pk_bf16(v[0], v[1]), tag, pk_bf16(v[2], v[3]), tag}, rs, o, h * 128, AUX_SC1);
    }
}

__global__ void __launch_bounds__(NWAVE * 64, 3) fused_kernel(Params p) {
    __shared__ __attribute__((aligned(1024))) unsigned char smem[LDS_BYTES];
    LAS unsigned char* L = (LAS unsigned char*)smem;
    LAS unsigned* FL = (LAS unsigned*)(L + FL_OFF);
    const int tid = threadIdx.x, lane = tid & 63, w = __builtin_amdgcn_readfirstlane(tid >> 6), bid = blockIdx.x;
    const int g = lane >> 4, c = lane & 15;
    const __amdgpu_buffer_rsrc_t rs = __builtin_amdgcn_make_buffer_rsrc((void*)p.ws, 0, WS_BYTES, 0x00020000);
    STAMP(0);
    constexpr float C2 = 0.08838834764831845f * 1.4426950408889634f;

    const int xcd = bid & 7, idx = bid >> 3;
    const bool has_w1 = bid < 224;
    const bool has_w2 = idx >= 4;
    const int ks_o = xcd * 8 + (idx >> 2), nsl_o = idx & 3;
    const int t2 = xcd * 28 + (idx - 4), ks_2 = t2 >> 2, nsl_2 = t2 & 3;
    const int gid = xcd * 4 + (idx >> 3), sp = idx & 7;
    const int g_wo = has_w1 ? 16 : 12, g_w2 = g_wo + 4;

    if (w != 0) {
        unsigned ep = 0;
        v4u xv[2];
        const GAS unsigned char* xp = (const GAS unsigned char*)p.x + (w - 1) * 2048 + lane * 16;
        xv[0] = *(const GAS v4u*)xp;
        xv[1] = *(const GAS v4u*)(xp + 1024);
        if (w == 8) ep = __hip_atomic_load((GAS unsigned*)(p.ws + WS_EPOCH), __ATOMIC_RELAXED, __HIP_MEMORY_SCOPE_AGENT);
        __builtin_amdgcn_s_barrier();
        ln_row(xv, L + XN_OFF + (w - 1) * XN_STR, lane);
        lds_inc(FL + FL_XN, lane);
        if (w == 8) {
            FL[FL_TV] = __builtin_amdgcn_readfirstlane(ep) + 1u;
            lds_set(FL + FL_T, 1);
        }
        STAMP(9);
    }
    if (w == 0) {
        FL[lane] = 0u;
        waitlds();
        int gi = 0;
        const unsigned fl_landed = FL_OFF + 4 * FL_LANDED;
        auto tile = [&](const void* base, int nbytes, int vb, int step, int mulA, int maskA, int maskB, int R) {
            const __amdgpu_buffer_rsrc_t rw = __builtin_amdgcn_make_buffer_rsrc((void*)base, 0, nbytes, 0x00020000);
#pragma nounroll
            for (int i0 = 0; i0 < 32; i0 += 8) {
#pragma unroll
                for (int j = 0; j < 8; ++j) {
                    const int i = i0 + j;
                    waitvm<MK_WIN - 1>();
                    if (j == 7 && gi >= MK_WIN / 8 - 1) lds_set_asm(fl_landed, gi - (MK_WIN / 8 - 1));
                    __builtin_amdgcn_raw_ptr_buffer_load_lds(rw, (LAS void*)(L + R + i * 1024), 16, vb ^ (((i * mulA) & maskA) | ((i * 32) & maskB)), i * step, 0, AUX_NT);
                }
                ++gi;
            }
        };
        const int l = lane;
        const size_t kvb = ((size_t)gid * SK + (SK - WIN) + sp * 128) * (DH * 2);
        {
            const int vb = ((bid & 3) * 256 + (l >> 3)) * (NQ * 2) + (bid >> 2) * 128 + (((l & 7) ^ (2 * ((l >> 4) & 1))) * 16);
            const __amdgpu_buffer_rsrc_t rw = __builtin_amdgcn_make_buffer_rsrc((void*)p.wq, 0, D * NQ * 2, 0x00020000);
#pragma unroll
            for (int i = 0; i < 16; ++i)
                __builtin_amdgcn_raw_ptr_buffer_load_lds(rw, (LAS void*)(L + R0 + i * 1024), 16, vb ^ ((i & 1) << 6), i * 8 * (NQ * 2), 0, AUX_NT);
            __builtin_amdgcn_s_barrier();
            gi = 2;
#pragma nounroll
            for (int i0 = 16; i0 < 32; i0 += 8) {
#pragma unroll
                for (int j = 0; j < 8; ++j) {
                    const int i = i0 + j;
                    waitvm<MK_WIN - 1>();
                    if (j == 7 && gi >= MK_WIN / 8 - 1) lds_set_asm(fl_landed, gi - (MK_WIN / 8 - 1));
                    __builtin_amdgcn_raw_ptr_buffer_load_lds(rw, (LAS void*)(L + R0 + i * 1024), 16, vb ^ ((i & 1) << 6), i * 8 * (NQ * 2), 0, AUX_NT);
                }
                ++gi;
            }
        }
        STAMP(1);
        tile((const unsigned char*)p.kc + kvb, 32768, (l >> 4) * 256 + (((l & 15) ^ (l >> 4)) * 16), 1024, 64, 0xC0, 0, R1);
        STAMP(2);
        tile((const unsigned char*)p.vc + kvb, 32768, (l >> 4) * 256 + (((l & 15) * 16) ^ ((l >> 4) << 5)), 1024, 128, 0x80, 0, R2);
        STAMP(3);
        if (has_w1) tile(p.w1, D * F * 2, ((bid & 3) * 256 + (l >> 3)) * (F * 2) + (bid >> 2) * 128 + (((l & 7) ^ (2 * ((l >> 4) & 1))) * 16), 8 * (F * 2), 64, 0x40, 0, R3);
        STAMP(4);
        lds_wait_ge_asm(FL_OFF + 4 * FL_R0, 4);
        tile(p.wo, NQ * D * 2, (ks_o * 64 + (l >> 5)) * 2048 + nsl_o * 512 + (((l & 31) * 16) ^ ((l >> 5) << 5)), 4096, 64, 0x40, 0x80, R0);
        STAMP(5);
        if (has_w2) {
            lds_wait_ge_asm(FL_OFF + 4 * FL_R1, 4);
            tile(p.w2, F * D * 2, (ks_2 * 64 + (l >> 5)) * 2048 + nsl_2 * 512 + (((l & 31) * 16) ^ ((l >> 5) << 5)), 4096, 64, 0x40, 0x80, R1);
        }
        STAMP(6);
        if constexpr (MK_WIN > 24) { waitvm<24>(); lds_set_asm(fl_landed, gi - 3); }
        if constexpr (MK_WIN > 16) { waitvm<16>(); lds_set_asm(fl_landed, gi - 2); }
        waitvm<8>(); lds_set_asm(fl_landed, gi - 1);
        waitvm<0>(); lds_set_asm(fl_landed, gi);
        STAMP(7);
    } else if (w <= 4) {
        const int xi = w - 1;
        unsigned tag;
        lds_wait_ge(FL + FL_T, 1);
        gemm_a(L, R0, FL, 4, FL + FL_R0, xi, bid & 3, lane, rs, QP_OFF + ((bid & 3) * 8 + (c & 7)) * QP_ROW + (bid >> 2) * 256, tag);
        lds_inc(FL + FL_QT, lane);
        STAMP(1);
        v4u kf[2][4], vf[8];
        lds_wait_ge(FL + FL_LANDED, 4 + xi + 1);
#pragma unroll
        for (int s = 0; s < 4; ++s) {
            const LAS unsigned char* kp = L + R1 + (32 * xi + c) * 256 + (((4 * s + g) ^ c) * 16);
            kf[0][s] = *(const LAS v4u*)kp;
            kf[1][s] = *(const LAS v4u*)(kp + 4096);
        }
        waitlds();
        lds_inc(FL + FL_R1, lane);
        lds_wait_ge(FL + FL_LANDED, 8 + xi + 1);
        {
            const int q = (lane & 15) >> 2, pp = lane & 3;
            const int a0 = R2 + (32 * xi + 4 * g + q) * 256 + (pp >> 1) * 16 + 8 * (pp & 1) + 16 * (2 * q + 8 * (g & 1));
#pragma unroll
            for (int dt = 0; dt < 8; ++dt) { const LAS unsigned char* vp = L + (a0 ^ (32 * dt)); vf[dt] = tr_frag(vp, vp + 4096); }
        }
        waitlds();
        lds_inc(FL + FL_R2, lane);
        STAMP(2);
        lds_wait_ge(FL + FL_Q, 4);
        STAMP(3);
        __builtin_amdgcn_s_setprio(2);
        v4u qf[4];
#pragma unroll
        for (int s = 0; s < 4; ++s) qf[s] = *(const LAS v4u*)(L + QS_OFF + (c & 7) * QS_STR + (32 * s + 8 * g) * 2);
        f32x4 S0 = {0.f, 0.f, 0.f, 0.f}, S1 = {0.f, 0.f, 0.f, 0.f};
#pragma unroll
        for (int s = 0; s < 4; ++s) { S0 = mfma(kf[0][s], qf[s], S0); S1 = mfma(kf[1][s], qf[s], S1); }
        float m = fmaxf(fmaxf(fmaxf(S0[0], S0[1]), fmaxf(S0[2], S0[3])), fmaxf(fmaxf(S1[0], S1[1]), fmaxf(S1[2], S1[3])));
        m = xrow16_max(m);
        asm volatile("" : "+v"(m));
        STAMP(10);
        const float mc = m * C2;
#pragma unroll
        for (int q = 0; q < 4; ++q) { S0[q] = __builtin_amdgcn_exp2f(fmaf(S0[q], C2, -mc)); S1[q] = __builtin_amdgcn_exp2f(fmaf(S1[q], C2, -mc)); }
        float lsum = ((S0[0] + S0[1]) + (S0[2] + S0[3])) + ((S1[0] + S1[1]) + (S1[2] + S1[3]));
        const v4u pb = {pk_bf16(S0[0], S0[1]), pk_bf16(S0[2], S0[3]), pk_bf16(S1[0], S1[1]), pk_bf16(S1[2], S1[3])};
        f32x4 O[8];
#pragma unroll
        for (int dt = 0; dt < 8; ++dt) O[dt] = mfma(vf[dt], pb, f32x4{0.f, 0.f, 0.f, 0.f});
        lsum = xrow16_sum(lsum);
        asm volatile("" : "+v"(lsum), "+v"(O[7]));
        STAMP(11);
        LAS float* ml = (LAS float*)(L + ML_OFF);
        if (lane < 8) { ml[xi * 16 + lane] = m; ml[xi * 16 + 8 + lane] = lsum; }
        lds_wait_ge(FL + FL_R2, 4);
#pragma unroll
        for (int h = 0; h < 4; ++h) {
            const f32x4 v = sel4(c >= 8, O[2 * h + 1], O[2 * h]);
            *(LAS f32x4*)(L + R2 + xi * 4096 + ((2 * h + (c >> 3)) * 32 + g * 8 + (c & 7)) * 16) = v;
        }
        lds_inc(FL + FL_MRG, lane);
        STAMP(12);
        lds_wait_ge_fast(FL + FL_MRG, 4);
        STAMP(13);
        {
            float mw[4], lw[4];
#pragma unroll
            for (int k = 0; k < 4; ++k) { mw[k] = ml[k * 16 + (c & 7)]; lw[k] = ml[k * 16 + 8 + (c & 7)]; }
            f32x4 ov[4];
#pragma unroll
            for (int k = 0; k < 4; ++k) ov[k] = *(const LAS f32x4*)(L + R2 + k * 4096 + ((2 * xi + (c >> 3)) * 32 + g * 8 + (c & 7)) * 16);
            const float M = fmaxf(fmaxf(mw[0], mw[1]), fmaxf(mw[2], mw[3]));
            float Lt = 0.f; f32x4 o = {0.f, 0.f, 0.f, 0.f};
#pragma unroll
            for (int k = 0; k < 4; ++k) { const float f = __builtin_amdgcn_exp2f((mw[k] - M) * C2); Lt = fmaf(f, lw[k], Lt); o += ov[k] * f; }
            __builtin_amdgcn_raw_buffer_store_b128(v4u{pk_bf16(o[0], o[1]), tag, pk_bf16(o[2], o[3]), tag}, rs,
                                                   OP_OFF + ((gid * 8 + sp) * 8 + (c & 7)) * 512 + (4 * (2 * xi + (c >> 3)) + g) * 16, 0, AUX_SC1);
            if (xi == 0 && lane < 8) __builtin_amdgcn_raw_buffer_store_b128(v4u{f2u(M), tag, f2u(Lt), tag}, rs, OM_OFF + ((gid * 8 + lane) * 8 + sp) * 16, 0, AUX_SC1);
        }
        STAMP(4);
        {
            const int head = ks_o >> 1, dh = ks_o & 1, hk = head >> 3, hh = head & 7;
            const int bb = 2 * xi + (lane >> 5), sh = (lane >> 4) & 1, gidb = bb * 4 + hk;
            int vo[5];
#pragma unroll
            for (int i = 0; i < 4; ++i) vo[i] = OP_OFF + ((gidb * 8 + 4 * sh + i) * 8 + hh) * 512 + (16 * dh + c) * 16;
            vo[4] = OM_OFF + ((gidb * 8 + hh) * 8 + (lane & 7)) * 16;
            __builtin_amdgcn_s_sleep(MK_DO);
            v4u o5[5];
            gather<5>(rs, vo, tag, o5);
            STAMP(5);
            const float ms = u2f(o5[4].x), ls = u2f(o5[4].z);
            float M = ms;
            M = fmaxf(M, dpp<XOR1>(M)); M = fmaxf(M, dpp<XOR2>(M)); M = fmaxf(M, dpp<XOR7>(M));
            const float we = __builtin_amdgcn_exp2f((ms - M) * C2);
            float Ls = we * ls;
            Ls += dpp<XOR1>(Ls); Ls += dpp<XOR2>(Ls); Ls += dpp<XOR7>(Ls);
            const float wn = we / Ls;
            LAS float* wl = (LAS float*)(L + ML_OFF + 256) + bb * 8;
            if ((lane & 31) < 8) wl[lane & 7] = wn;
            CBAR();
            const f32x4 wa = *(const LAS f32x4*)(wl + 4 * sh);
            float v0 = 0.f, v1 = 0.f, v2 = 0.f, v3 = 0.f;
#pragma unroll
            for (int i = 0; i < 4; ++i) {
                v0 = fmaf(wa[i], bf_lo(o5[i].x), v0); v1 = fmaf(wa[i], bf_hi(o5[i].x), v1);
                v2 = fmaf(wa[i], bf_lo(o5[i].z), v2); v3 = fmaf(wa[i], bf_hi(o5[i].z), v3);
            }
            v0 = x16_sum(v0); v1 = x16_sum(v1); v2 = x16_sum(v2); v3 = x16_sum(v3);
            if (sh == 0) *(LAS v2u*)(L + OA_OFF + bb * AC_STR + c * 8) = v2u{pk_bf16(v0, v1), pk_bf16(v2, v3)};
            lds_inc(FL + FL_O, lane);
        }
        STAMP(6);
        gemm_b(L, R0, FL, g_wo + 4, FL + FL_O, OA_OFF, xi, lane, rs, SP_OFF + (nsl_o * 64 + ks_o) * 8192, tag);
        STAMP(7);
        if (xi == 0) {
            const int nsl = bid & 3, j = bid >> 2, uo = (j >> 3) * 1024 + ((j & 7) * 8 + (lane & 7)) * 16;
            int vo[8];
#pragma unroll
            for (int i = 0; i < 8; ++i) vo[i] = SP_OFF + (nsl * 64 + 8 * i + (lane >> 3)) * 8192 + uo;
            __builtin_amdgcn_s_sleep(MK_DS);
            v4u f8[8];
            gather<8>(rs, vo, tag, f8);
            float sv[4] = {0.f, 0.f, 0.f, 0.f};
#pragma unroll
            for (int i = 0; i < 8; ++i) { sv[0] += bf_lo(f8[i].x); sv[1] += bf_hi(f8[i].x); sv[2] += bf_lo(f8[i].z); sv[3] += bf_hi(f8[i].z); }
#pragma unroll
            for (int q = 0; q < 4; ++q) { sv[q] += dpp<XOR8>(sv[q]); sv[q] = xrow16_sum(sv[q]); }
            if (lane < 8) *(LAS f32x4*)(L + ML_OFF + 512 + lane * 16) = f32x4{sv[0], sv[1], sv[2], sv[3]};
            lds_set(FL + FL_S, 1);
            STAMP(8);
        }
    } else {
        const int yi = w - 5;
        lds_wait_ge(FL + FL_T, 1);
        unsigned tag = __builtin_amdgcn_readfirstlane(FL[FL_TV]);
        {
            const int bq = gid >> 2, hk = gid & 3;
            int vo[4];
#pragma unroll
            for (int i = 0; i < 4; ++i) vo[i] = QP_OFF + (i * 8 + bq) * QP_ROW + (hk * 256 + yi * 64 + lane) * 16;
            lds_wait_ge(FL + FL_QT, 4);
            __builtin_amdgcn_s_sleep(MK_DQ);
            __builtin_amdgcn_s_setprio(2);
            v4u q4[4];
            gather<4>(rs, vo, tag, q4);
            const float v0 = (bf_lo(q4[0].x) + bf_lo(q4[1].x)) + (bf_lo(q4[2].x) + bf_lo(q4[3].x));
            const float v1 = (bf_hi(q4[0].x) + bf_hi(q4[1].x)) + (bf_hi(q4[2].x) + bf_hi(q4[3].x));
            const float v2 = (bf_lo(q4[0].z) + bf_lo(q4[1].z)) + (bf_lo(q4[2].z) + bf_lo(q4[3].z));
            const float v3 = (bf_hi(q4[0].z) + bf_hi(q4[1].z)) + (bf_hi(q4[2].z) + bf_hi(q4[3].z));
            const int n = (yi * 64 + lane) * 4;
            *(LAS v2u*)(L + QS_OFF + (n >> 7) * QS_STR + (n & 127) * 2) = v2u{pk_bf16(v0, v1), pk_bf16(v2, v3)};
            lds_inc(FL + FL_Q, lane);
            __builtin_amdgcn_s_setprio(0);
            STAMP(2);
        }
        if (has_w1) gemm_a(L, R3, FL, 16, nullptr, yi, bid & 3, lane, rs, HP_OFF + ((bid & 3) * 8 + (c & 7)) * HP_ROW + (bid >> 2) * 256, tag);
        else lds_wait_ge(FL + FL_LANDED, 14);
        STAMP(3);
        if (has_w2) {
            const int bb = 2 * yi + (lane >> 5), ph = (lane >> 4) & 1;
            const v2u bi = *(const GAS v2u*)(p.b1 + ks_2 * 64 + 4 * c);
            int vo[2];
#pragma unroll
            for (int i = 0; i < 2; ++i) vo[i] = HP_OFF + ((2 * ph + i) * 8 + bb) * HP_ROW + ks_2 * 256 + c * 16;
            __builtin_amdgcn_s_sleep(MK_DH);
            __builtin_amdgcn_s_setprio(2);
            v4u h2[2];
            gather<2>(rs, vo, tag, h2);
            STAMP(4);
            const float v0 = x16_sum(bf_lo(h2[0].x) + bf_lo(h2[1].x)) + bf_lo(bi.x), v1 = x16_sum(bf_hi(h2[0].x) + bf_hi(h2[1].x)) + bf_hi(bi.x);
            const float v2 = x16_sum(bf_lo(h2[0].z) + bf_lo(h2[1].z)) + bf_lo(bi.y), v3 = x16_sum(bf_hi(h2[0].z) + bf_hi(h2[1].z)) + bf_hi(bi.y);
            const v2u hv = {pk_bf16(gelu_tanh(v0), gelu_tanh(v1)), pk_bf16(gelu_tanh(v2), gelu_tanh(v3))};
            if (ph == 0) *(LAS v2u*)(L + HA_OFF + bb * AC_STR + c * 8) = hv;
            lds_inc(FL + FL_H, lane);
            STAMP(5);
            gemm_b(L, R1, FL, g_w2 + 4, FL + FL_H, HA_OFF, yi, lane, rs, YP_OFF + (nsl_2 * 56 + ks_2) * 8192, tag);
        } else {
            lds_wait_ge(FL + FL_LANDED, g_wo + 4);
        }
        STAMP(6);
        if (yi == 0) {
            const int nsl = bid & 3, j = bid >> 2, uo = (j >> 3) * 1024 + ((j & 7) * 8 + (lane & 7)) * 16;
            const int b = j >> 3, n = 256 * nsl + (j & 7) * 32 + 4 * (lane & 7);
            const v2u xx = *(const GAS v2u*)(p.x + b * D + n), bb = *(const GAS v2u*)(p.b2 + n);
            int vo[7];
#pragma unroll
            for (int i = 0; i < 7; ++i) vo[i] = YP_OFF + (nsl * 56 + 8 * i + (lane >> 3)) * 8192 + uo;
            __builtin_amdgcn_s_sleep(MK_DY);
            v4u f7[7];
            gather<7>(rs, vo, tag, f7);
            STAMP(7);
            float yv[4] = {0.f, 0.f, 0.f, 0.f};
#pragma unroll
            for (int i = 0; i < 7; ++i) { yv[0] += bf_lo(f7[i].x); yv[1] += bf_hi(f7[i].x); yv[2] += bf_lo(f7[i].z); yv[3] += bf_hi(f7[i].z); }
#pragma unroll
            for (int q = 0; q < 4; ++q) { yv[q] += dpp<XOR8>(yv[q]); yv[q] = xrow16_sum(yv[q]); }
            const float g0 = silu(yv[0] + bf_lo(bb.x)), g1 = silu(yv[1] + bf_hi(bb.x)), g2 = silu(yv[2] + bf_lo(bb.y)), g3 = silu(yv[3] + bf_hi(bb.y));
            lds_wait_ge(FL + FL_S, 1);
            const f32x4 sv = *(const LAS f32x4*)(L + ML_OFF + 512 + (lane & 7) * 16);
            if (lane < 8)
                *(GAS v2u*)(p.out + b * D + n) = v2u{pk_bf16(bf_lo(xx.x) + sv[0] * g0, bf_hi(xx.x) + sv[1] * g1), pk_bf16(bf_lo(xx.y) + sv[2] * g2, bf_hi(xx.y) + sv[3] * g3)};
            if (bid == 0 && lane == 0) __hip_atomic_store((GAS unsigned*)(p.ws + WS_EPOCH), tag, __ATOMIC_RELAXED, __HIP_MEMORY_SCOPE_AGENT);
            STAMP(8);
        }
    }
}

}

extern "C" void kernel_launch(void* const* d_in, const int* in_sizes, int n_in, void* d_out, int out_size, void* d_ws, size_t ws_size,
                              hipStream_t stream) {
    mk::Params p;
    p.x = (const u16*)d_in[0];
    p.kc = (const u16*)d_in[1];
    p.vc = (const u16*)d_in[2];
    p.wq = (const u16*)d_in[3];
    p.wo = (const u16*)d_in[4];
    p.w1 = (const u16*)d_in[6];
    p.b1 = (const u16*)d_in[7];
    p.w2 = (const u16*)d_in[8];
    p.b2 = (const u16*)d_in[9];
    p.out = (u16*)d_out;
    p.ws = (unsigned char*)d_ws;
    mk::fused_kernel<<<256, mk::NWAVE * 64, 0, stream>>>(p);
}
```

```cpp
#include <hip/hip_runtime.h>
#include <hip/hip_bf16.h>
#include <stdint.h>

#define GAS __attribute__((address_space(1)))
#define LAS __attribute__((address_space(3)))
typedef unsigned short u16;
typedef unsigned v4u __attribute__((ext_vector_type(4)));
typedef unsigned v2u __attribute__((ext_vector_type(2)));
typedef short v4s __attribute__((ext_vector_type(4)));
typedef float f32x4 __attribute__((ext_vector_type(4)));
typedef float f32x2 __attribute__((ext_vector_type(2)));
typedef __bf16 bf16x8 __attribute__((ext_vector_type(8)));
typedef __bf16 bf16x2 __attribute__((ext_vector_type(2)));

namespace mk {

constexpr int B = 8, D = 1024, SK = 4096, DH = 128, WIN = 1024, F = 3584, NQ = 4096;
constexpr int NWAVE = 9;
constexpr int MK_WIN = 24;
constexpr unsigned MK_TQ = 510, MK_TO = 830, MK_TH = 930, MK_TS = 1140, MK_TY = 1160;

constexpr int WS_EPOCH = 0;
constexpr int QP_ROW = 16384, QP_OFF = 4096;
constexpr int HP_ROW = 14336, HP_OFF = QP_OFF + 32 * QP_ROW;
constexpr int OP_OFF = HP_OFF + 32 * HP_ROW;
constexpr int OM_OFF = OP_OFF + 32 * 8 * 8 * 512;
constexpr int SP_OFF = OM_OFF + 32 * 8 * 8 * 16;
constexpr int YP_OFF = SP_OFF + 4 * 64 * 8192;
constexpr int WS_END = YP_OFF + 4 * 56 * 8192;
constexpr int WS_BYTES = WS_END;

constexpr int R0 = 0, R1 = 32768, R2 = 65536, R3 = 98304;
constexpr int XN_STR = 2048 + 16, XN_OFF = 131072;
constexpr int QS_STR = 256 + 16, QS_OFF = XN_OFF + 8 * XN_STR;
constexpr int AC_STR = 128 + 16, OA_OFF = QS_OFF + 8 * QS_STR;
constexpr int HA_OFF = OA_OFF + 8 * AC_STR;
constexpr int ML_OFF = HA_OFF + 8 * AC_STR;
constexpr int FL_OFF = ML_OFF + 1024;
constexpr int LDS_USED = FL_OFF + 256;
constexpr int LDS_BYTES = LDS_USED;
static_assert(LDS_BYTES <= 160 * 1024 && (ML_OFF % 16) == 0 && (OA_OFF % 16) == 0 && (QS_OFF % 16) == 0, "lds");
enum { FL_LANDED = 0, FL_R0 = 1, FL_R1 = 2, FL_XN = 3, FL_QT = 4, FL_Q = 5, FL_MRG = 6, FL_O = 7, FL_H = 8, FL_S = 9, FL_T = 10, FL_TV = 11 };

struct Params {
    const u16 *x, *kc, *vc, *wq, *wo, *w1, *b1, *w2, *b2;
    u16* out;
    unsigned char* ws;
};

constexpr unsigned kSpinCap = 1u << 17;
constexpr int AUX_NT = 2, AUX_SC1 = 16;

__device__ __forceinline__ float bf_lo(unsigned u) { return __builtin_bit_cast(float, u << 16); }
__device__ __forceinline__ float bf_hi(unsigned u) { return __builtin_bit_cast(float, u & 0xffff0000u); }
__device__ __forceinline__ unsigned pk_bf16(float lo, float hi) {
    f32x2 v = {lo, hi};
    return __builtin_bit_cast(unsigned, __builtin_convertvector(v, bf16x2));
}
__device__ __forceinline__ f32x4 mfma(v4u a, v4u b, f32x4 c) {
    return __builtin_amdgcn_mfma_f32_16x16x32_bf16(__builtin_bit_cast(bf16x8, a), __builtin_bit_cast(bf16x8, b), c, 0, 0, 0);
}
__device__ __forceinline__ unsigned f2u(float f) { return __builtin_bit_cast(unsigned, f); }
__device__ __forceinline__ float u2f(unsigned u) { return __builtin_bit_cast(float, u); }

#define CBAR() asm volatile("" ::: "memory")
template <int N> __device__ __forceinline__ void waitvm() { asm volatile("s_waitcnt vmcnt(%0)" ::"i"(N) : "memory"); }
__device__ __forceinline__ void waitlds() { asm volatile("s_waitcnt lgkmcnt(0)" ::: "memory"); }

__device__ __forceinline__ void lds_set(LAS unsigned* p, unsigned v) {
    CBAR();
    __hip_atomic_store(p, v, __ATOMIC_RELAXED, __HIP_MEMORY_SCOPE_WORKGROUP);
    CBAR();
}
__device__ __forceinline__ void lds_inc(LAS unsigned* p, int lane) {
    CBAR();
    if (lane == 0) __hip_atomic_fetch_add(p, 1u, __ATOMIC_RELAXED, __HIP_MEMORY_SCOPE_WORKGROUP);
    CBAR();
}
__device__ __forceinline__ unsigned lds_get(LAS unsigned* p) {
    return (unsigned)__builtin_amdgcn_readfirstlane(__hip_atomic_load(p, __ATOMIC_RELAXED, __HIP_MEMORY_SCOPE_WORKGROUP));
}
__device__ __forceinline__ void lds_wait_ge(LAS unsigned* p, unsigned v) {
#pragma nounroll
    for (unsigned spins = 0; spins < kSpinCap; ++spins) {
        if (lds_get(p) >= v) break;
        __builtin_amdgcn_s_sleep(1);
    }
    CBAR();
}

__device__ __forceinline__ void lds_wait_ge_fast(LAS unsigned* p, unsigned v) {
#pragma nounroll
    for (unsigned spins = 0; spins < 4u * kSpinCap; ++spins) {
        if (lds_get(p) >= v) break;
    }
    CBAR();
}
__device__ __forceinline__ void lds_set_asm(unsigned addr, unsigned v) {
    asm volatile("ds_write_b32 %0, %1" ::"v"(addr), "v"(v) : "memory");
}
__device__ __forceinline__ void lds_wait_ge_asm(unsigned addr, unsigned v) {
#pragma nounroll
    for (unsigned spins = 0; spins < kSpinCap; ++spins) {
        unsigned x;
        asm volatile("ds_read_b32 %0, %1\n\ts_waitcnt lgkmcnt(0)" : "=v"(x) : "v"(addr) : "memory");
        if ((unsigned)__builtin_amdgcn_readfirstlane(x) >= v) break;
        __builtin_amdgcn_s_sleep(1);
    }
}
__device__ __forceinline__ float xrow16_max(float x) {
    auto s = __builtin_amdgcn_permlane16_swap(f2u(x), f2u(x), false, false);
    x = fmaxf(u2f(s[0]), u2f(s[1]));
    auto t = __builtin_amdgcn_permlane32_swap(f2u(x), f2u(x), false, false);
    return fmaxf(u2f(t[0]), u2f(t[1]));
}
__device__ __forceinline__ float x16_sum(float x) {
    auto s = __builtin_amdgcn_permlane16_swap(f2u(x), f2u(x), false, false);
    return u2f(s[0]) + u2f(s[1]);
}
__device__ __forceinline__ float x32_sum(float x) {
    auto t = __builtin_amdgcn_permlane32_swap(f2u(x), f2u(x), false, false);
    return u2f(t[0]) + u2f(t[1]);
}
__device__ __forceinline__ float xrow16_sum(float x) { return x32_sum(x16_sum(x)); }
template <int CTRL> __device__ __forceinline__ float dpp(float x) {
    return u2f(__builtin_amdgcn_mov_dpp(f2u(x), CTRL, 0xf, 0xf, true));
}
constexpr int XOR1 = 0xB1, XOR2 = 0x4E, XOR7 = 0x141, XOR8 = 0x128;
__device__ __forceinline__ float row16_sum(float x) {
    x += dpp<XOR1>(x); x += dpp<XOR2>(x); x += dpp<XOR7>(x); x += dpp<XOR8>(x);
    return x;
}
__device__ __forceinline__ float wave_sum(float x) { return xrow16_sum(row16_sum(x)); }

__device__ __forceinline__ f32x4 sel4(bool hi, f32x4 a_hi, f32x4 a_lo) {
    asm volatile("" : "+v"(a_hi), "+v"(a_lo));
    return f32x4{hi ? a_hi[0] : a_lo[0], hi ? a_hi[1] : a_lo[1], hi ? a_hi[2] : a_lo[2], hi ? a_hi[3] : a_lo[3]};
}
__device__ __forceinline__ void wait_until(unsigned long long t0, unsigned ticks) {
    __builtin_amdgcn_s_sleep(8);
#pragma nounroll
    for (unsigned spins = 0; spins < 4096u; ++spins) {
        if (__builtin_amdgcn_s_memrealtime() - t0 >= ticks) break;
        __builtin_amdgcn_s_sleep(1);
    }
}
__device__ __forceinline__ v4u tr_frag(const LAS unsigned char* p0, const LAS unsigned char* p1) {
    const v4s a = __builtin_amdgcn_ds_read_tr16_b64_v4i16((LAS v4s*)p0);
    const v4s b = __builtin_amdgcn_ds_read_tr16_b64_v4i16((LAS v4s*)p1);
    const v2u x = __builtin_bit_cast(v2u, a), y = __builtin_bit_cast(v2u, b);
    return v4u{x.x, x.y, y.x, y.y};
}

template <int N>
__device__ __forceinline__ void gather(__amdgpu_buffer_rsrc_t rs, const int (&voff)[N], unsigned tag, v4u (&g)[N]) {
    unsigned pend = (1u << N) - 1u;
#pragma nounroll
    for (unsigned spins = 0; spins < kSpinCap; ++spins) {
        CBAR();
#pragma unroll
        for (int i = 0; i < N; ++i)
            if (pend & (1u << i)) g[i] = __builtin_amdgcn_raw_buffer_load_b128(rs, voff[i], 0, AUX_SC1);
#pragma unroll
        for (int i = 0; i < N; ++i)
            if ((pend & (1u << i)) && __all(g[i].y == tag && g[i].w == tag)) pend &= ~(1u << i);
        if (pend == 0u) break;
    }
    CBAR();
}

__device__ __forceinline__ void ln_row(const v4u (&xv)[2], LAS unsigned char* dst, int lane) {
    float v[16];
#pragma unroll
    for (int q = 0; q < 4; ++q) {
        v[2 * q] = bf_lo(xv[0][q]); v[2 * q + 1] = bf_hi(xv[0][q]);
        v[8 + 2 * q] = bf_lo(xv[1][q]); v[8 + 2 * q + 1] = bf_hi(xv[1][q]);
    }
    float s4[4], t4[4];
#pragma unroll
    for (int q = 0; q < 4; ++q) s4[q] = (v[4 * q] + v[4 * q + 1]) + (v[4 * q + 2] + v[4 * q + 3]);
    const float mean = wave_sum((s4[0] + s4[1]) + (s4[2] + s4[3])) * (1.0f / D);
#pragma unroll
    for (int i = 0; i < 16; ++i) v[i] -= mean;
#pragma unroll
    for (int q = 0; q < 4; ++q) t4[q] = fmaf(v[4 * q], v[4 * q], v[4 * q + 1] * v[4 * q + 1]) + fmaf(v[4 * q + 2], v[4 * q + 2], v[4 * q + 3] * v[4 * q + 3]);
    const float var = wave_sum((t4[0] + t4[1]) + (t4[2] + t4[3])) * (1.0f / D);
    const float rs_ = 1.0f / sqrtf(var + 1e-5f);
#pragma unroll
    for (int h = 0; h < 2; ++h) {
        const v4u o = {pk_bf16(v[h * 8] * rs_, v[h * 8 + 1] * rs_), pk_bf16(v[h * 8 + 2] * rs_, v[h * 8 + 3] * rs_),
                       pk_bf16(v[h * 8 + 4] * rs_, v[h * 8 + 5] * rs_), pk_bf16(v[h * 8 + 6] * rs_, v[h * 8 + 7] * rs_)};
        *(LAS v4u*)(dst + h * 1024 + lane * 16) = o;
    }
}

__device__ __forceinline__ float gelu_tanh(float s) {
    const float u = 0.7978845608028654f * (s + 0.044715f * s * s * s);
    const float e = __expf(2.0f * u);
    const float th = 1.0f - 2.0f / (e + 1.0f);
    return 0.5f * s * (1.0f + th);
}
__device__ __forceinline__ float silu(float y) { return y / (1.0f + __expf(-y)); }

__device__ __forceinline__ void gemm_a(LAS unsigned char* L, int R, LAS unsigned* FL, int need_landed, LAS unsigned* free_flag, int nt, int ks, int lane,
                                       __amdgpu_buffer_rsrc_t rs, int out_off, unsigned& tag) {
    const int g = lane >> 4, q = (lane & 15) >> 2, p = lane & 3, c = lane & 15;
    const LAS unsigned char* ap = L + R + (8 * g + q) * 128 + (((2 * nt + (p >> 1)) ^ (2 * ((q >> 1) & 1) + 4 * (g & 1))) * 16) + 8 * (p & 1);
    const LAS unsigned char* bp = L + XN_OFF + (c & 7) * XN_STR + (ks * 256 + 8 * g) * 2;
    lds_wait_ge(FL + FL_XN, 8);
    v4u bx[8];
#pragma unroll
    for (int s = 0; s < 8; ++s) bx[s] = *(const LAS v4u*)(bp + s * 64);
    lds_wait_ge(FL + FL_LANDED, need_landed);
    v4u a[8];
#pragma unroll
    for (int s = 0; s < 8; ++s) a[s] = tr_frag(ap + s * 4096, ap + s * 4096 + 512);
    f32x4 acc0 = {0.f, 0.f, 0.f, 0.f}, acc1 = {0.f, 0.f, 0.f, 0.f};
#pragma unroll
    for (int s = 0; s < 8; s += 2) { acc0 = mfma(a[s], bx[s], acc0); acc1 = mfma(a[s + 1], bx[s + 1], acc1); }
    acc0 += acc1;
    lds_wait_ge(FL + FL_T, 1);
    tag = __builtin_amdgcn_readfirstlane(FL[FL_TV]);
    if (c < 8) __builtin_amdgcn_raw_buffer_store_b128(v4u{pk_bf16(acc0[0], acc0[1]), tag, pk_bf16(acc0[2], acc0[3]), tag}, rs, out_off + (4 * nt + g) * 16, 0, AUX_SC1);
    if (free_flag) lds_inc(free_flag, lane);
}
__device__ __forceinline__ void gemm_b(LAS unsigned char* L, int R, LAS unsigned* FL, int need_landed, LAS unsigned* act_flag, int act_off, int xi, int lane,
                                       __amdgpu_buffer_rsrc_t rs, int out_off, unsigned tag) {
    const int g = lane >> 4, q = (lane & 15) >> 2, p = lane & 3, c = lane & 15;
    const LAS unsigned char* rowp = L + R + (8 * g + q) * 512 + (p >> 1) * 16 + 8 * (p & 1);
    lds_wait_ge(FL + FL_LANDED, need_landed);
    v4u a[4][2];
#pragma unroll
    for (int n = 0; n < 4; ++n) {
        const LAS unsigned char* ap = rowp + (((8 * xi + 2 * n) ^ (2 * q + 8 * (g & 1))) * 16);
#pragma unroll
        for (int s = 0; s < 2; ++s) a[n][s] = tr_frag(ap + s * 16384, ap + s * 16384 + 2048);
    }
    lds_wait_ge_fast(act_flag, 4);
    const LAS unsigned char* bp = L + act_off + (c & 7) * AC_STR + g * 16;
    const v4u b0 = *(const LAS v4u*)bp, b1 = *(const LAS v4u*)(bp + 64);
    f32x4 acc[4];
#pragma unroll
    for (int n = 0; n < 4; ++n) acc[n] = mfma(a[n][0], b0, f32x4{0.f, 0.f, 0.f, 0.f});
#pragma unroll
    for (int n = 0; n < 4; ++n) acc[n] = mfma(a[n][1], b1, acc[n]);
    const bool hi = c >= 8;
    const int o = out_off + (c & 7) * 1024 + (16 * xi + (hi ? 4 : 0) + g) * 16;
#pragma unroll
    for (int h = 0; h < 2; ++h) {
        const f32x4 v = sel4(hi, acc[2 * h + 1], acc[2 * h]);
        __builtin_amdgcn_raw_buffer_store_b128(v4u{pk_bf16(v[0], v[1]), tag, pk_bf16(v[2], v[3]), tag}, rs, o, h * 128, AUX_SC1);
    }
}

__global__ void __launch_bounds__(NWAVE * 64, 3) fused_kernel(Params p) {
    __shared__ __attribute__((aligned(1024))) unsigned char smem[LDS_BYTES];
    LAS unsigned char* L = (LAS unsigned char*)smem;
    LAS unsigned* FL = (LAS unsigned*)(L + FL_OFF);
    const int tid = threadIdx.x, lane = tid & 63, w = __builtin_amdgcn_readfirstlane(tid >> 6), bid = blockIdx.x;
    const int g = lane >> 4, c = lane & 15;
    const __amdgpu_buffer_rsrc_t rs = __builtin_amdgcn_make_buffer_rsrc((void*)p.ws, 0, WS_BYTES, 0x00020000);
    const unsigned long long tstart = __builtin_amdgcn_s_memrealtime();
    constexpr float C2 = 0.08838834764831845f * 1.4426950408889634f;

    const int xcd = bid & 7, idx = bid >> 3;
    const bool has_w1 = bid < 224;
    const bool has_w2 = idx >= 4;
    const int ks_o = xcd * 8 + (idx >> 2), nsl_o = idx & 3;
    const int t2 = xcd * 28 + (idx - 4), ks_2 = t2 >> 2, nsl_2 = t2 & 3;
    const int gid = xcd * 4 + (idx >> 3), sp = idx & 7;
    const int g_wo = has_w1 ? 16 : 12, g_w2 = g_wo + 4;

    if (w != 0) {
        unsigned ep = 0;
        v4u xv[2];
        const GAS unsigned char* xp = (const GAS unsigned char*)p.x + (w - 1) * 2048 + lane * 16;
        xv[0] = *(const GAS v4u*)xp;
        xv[1] = *(const GAS v4u*)(xp + 1024);
        if (w == 8) ep = __hip_atomic_load((GAS unsigned*)(p.ws + WS_EPOCH), __ATOMIC_RELAXED, __HIP_MEMORY_SCOPE_AGENT);
        __builtin_amdgcn_s_barrier();
        ln_row(xv, L + XN_OFF + (w - 1) * XN_STR, lane);
        lds_inc(FL + FL_XN, lane);
        if (w == 8) {
            FL[FL_TV] = __builtin_amdgcn_readfirstlane(ep) + 1u;
            lds_set(FL + FL_T, 1);
        }
    }
    if (w == 0) {
        FL[lane] = 0u;
        waitlds();
        int gi = 0;
        const unsigned fl_landed = FL_OFF + 4 * FL_LANDED;
        auto tile = [&](const void* base, int nbytes, int vb, int step, int mulA, int maskA, int maskB, int R) {
            const __amdgpu_buffer_rsrc_t rw = __builtin_amdgcn_make_buffer_rsrc((void*)base, 0, nbytes, 0x00020000);
#pragma nounroll
            for (int i0 = 0; i0 < 32; i0 += 8) {
#pragma unroll
                for (int j = 0; j < 8; ++j) {
                    const int i = i0 + j;
                    waitvm<MK_WIN - 1>();
                    if (j == 7 && gi >= MK_WIN / 8 - 1) lds_set_asm(fl_landed, gi - (MK_WIN / 8 - 1));
                    __builtin_amdgcn_raw_ptr_buffer_load_lds(rw, (LAS void*)(L + R + i * 1024), 16, vb ^ (((i * mulA) & maskA) | ((i * 32) & maskB)), i * step, 0, AUX_NT);
                }
                ++gi;
            }
        };
        const int l = lane;
        const size_t kvb = ((size_t)gid * SK + (SK - WIN) + sp * 128) * (DH * 2);
        {
            const int vb = ((bid & 3) * 256 + (l >> 3)) * (NQ * 2) + (bid >> 2) * 128 + (((l & 7) ^ (2 * ((l >> 4) & 1))) * 16);
            const __amdgpu_buffer_rsrc_t rw = __builtin_amdgcn_make_buffer_rsrc((void*)p.wq, 0, D * NQ * 2, 0x00020000);
#pragma unroll
            for (int i = 0; i < 16; ++i)
                __builtin_amdgcn_raw_ptr_buffer_load_lds(rw, (LAS void*)(L + R0 + i * 1024), 16, vb ^ ((i & 1) << 6), i * 8 * (NQ * 2), 0, AUX_NT);
            __builtin_amdgcn_s_barrier();
            gi = 2;
#pragma nounroll
            for (int i0 = 16; i0 < 32; i0 += 8) {
#pragma unroll
                for (int j = 0; j < 8; ++j) {
                    const int i = i0 + j;
                    waitvm<MK_WIN - 1>();
                    if (j == 7 && gi >= MK_WIN / 8 - 1) lds_set_asm(fl_landed, gi - (MK_WIN / 8 - 1));
                    __builtin_amdgcn_raw_ptr_buffer_load_lds(rw, (LAS void*)(L + R0 + i * 1024), 16, vb ^ ((i & 1) << 6), i * 8 * (NQ * 2), 0, AUX_NT);
                }
                ++gi;
            }
        }
        tile((const unsigned char*)p.kc + kvb, 32768, (l >> 4) * 256 + (((l & 15) ^ (l >> 4)) * 16), 1024, 64, 0xC0, 0, R1);
        tile((const unsigned char*)p.vc + kvb, 32768, (l >> 4) * 256 + (((l & 15) * 16) ^ ((l >> 4) << 5)), 1024, 128, 0x80, 0, R2);
        if (has_w1) tile(p.w1, D * F * 2, ((bid & 3) * 256 + (l >> 3)) * (F * 2) + (bid >> 2) * 128 + (((l & 7) ^ (2 * ((l >> 4) & 1))) * 16), 8 * (F * 2), 64, 0x40, 0, R3);
        lds_wait_ge_asm(FL_OFF + 4 * FL_R0, 4);
        tile(p.wo, NQ * D * 2, (ks_o * 64 + (l >> 5)) * 2048 + nsl_o * 512 + (((l & 31) * 16) ^ ((l >> 5) << 5)), 4096, 64, 0x40, 0x80, R0);
        if (has_w2) {
            lds_wait_ge_asm(FL_OFF + 4 * FL_R1, 4);
            tile(p.w2, F * D * 2, (ks_2 * 64 + (l >> 5)) * 2048 + nsl_2 * 512 + (((l & 31) * 16) ^ ((l >> 5) << 5)), 4096, 64, 0x40, 0x80, R1);
        }
        waitvm<16>(); lds_set_asm(fl_landed, gi - 2);
        waitvm<8>(); lds_set_asm(fl_landed, gi - 1);
        waitvm<0>(); lds_set_asm(fl_landed, gi);
    } else if (w <= 4) {
        const int xi = w - 1;
        unsigned tag;
        gemm_a(L, R0, FL, 4, FL + FL_R0, xi, bid & 3, lane, rs, QP_OFF + ((bid & 3) * 8 + (c & 7)) * QP_ROW + (bid >> 2) * 256, tag);
        lds_inc(FL + FL_QT, lane);
        v4u kf[2][4], vf[8];
        lds_wait_ge(FL + FL_LANDED, 4 + xi + 1);
#pragma unroll
        for (int s = 0; s < 4; ++s) {
            const LAS unsigned char* kp = L + R1 + (32 * xi + c) * 256 + (((4 * s + g) ^ c) * 16);
            kf[0][s] = *(const LAS v4u*)kp;
            kf[1][s] = *(const LAS v4u*)(kp + 4096);
        }
        waitlds();
        lds_inc(FL + FL_R1, lane);
        lds_wait_ge(FL + FL_LANDED, 8 + xi + 1);
        {
            const int q = (lane & 15) >> 2, pp = lane & 3;
            const int a0 = R2 + (32 * xi + 4 * g + q) * 256 + (pp >> 1) * 16 + 8 * (pp & 1) + 16 * (2 * q + 8 * (g & 1));
#pragma unroll
            for (int dt = 0; dt < 8; ++dt) { const LAS unsigned char* vp = L + (a0 ^ (32 * dt)); vf[dt] = tr_frag(vp, vp + 4096); }
        }
        lds_wait_ge_fast(FL + FL_Q, 4);
        __builtin_amdgcn_s_setprio(2);
        v4u qf[4];
#pragma unroll
        for (int s = 0; s < 4; ++s) qf[s] = *(const LAS v4u*)(L + QS_OFF + (c & 7) * QS_STR + (32 * s + 8 * g) * 2);
        f32x4 S0 = {0.f, 0.f, 0.f, 0.f}, S1 = {0.f, 0.f, 0.f, 0.f};
#pragma unroll
        for (int s = 0; s < 4; ++s) { S0 = mfma(kf[0][s], qf[s], S0); S1 = mfma(kf[1][s], qf[s], S1); }
        float m = fmaxf(fmaxf(fmaxf(S0[0], S0[1]), fmaxf(S0[2], S0[3])), fmaxf(fmaxf(S1[0], S1[1]), fmaxf(S1[2], S1[3])));
        m = xrow16_max(m);
        const float mc = m * C2;
#pragma unroll
        for (int q = 0; q < 4; ++q) { S0[q] = __builtin_amdgcn_exp2f(fmaf(S0[q], C2, -mc)); S1[q] = __builtin_amdgcn_exp2f(fmaf(S1[q], C2, -mc)); }
        float lsum = ((S0[0] + S0[1]) + (S0[2] + S0[3])) + ((S1[0] + S1[1]) + (S1[2] + S1[3]));
        const v4u pb = {pk_bf16(S0[0], S0[1]), pk_bf16(S0[2], S0[3]), pk_bf16(S1[0], S1[1]), pk_bf16(S1[2], S1[3])};
        f32x4 O[8];
#pragma unroll
        for (int dt = 0; dt < 8; ++dt) O[dt] = mfma(vf[dt], pb, f32x4{0.f, 0.f, 0.f, 0.f});
        lsum = xrow16_sum(lsum);
        LAS float* ml = (LAS float*)(L + ML_OFF);
        if (lane < 8) { ml[xi * 16 + lane] = m; ml[xi * 16 + 8 + lane] = lsum; }
#pragma unroll
        for (int h = 0; h < 4; ++h) {
            const f32x4 v = sel4(c >= 8, O[2 * h + 1], O[2 * h]);
            *(LAS f32x4*)(L + R2 + xi * 8192 + ((2 * h + (c >> 3)) * 32 + g * 8 + (c & 7)) * 16) = v;
        }
        lds_inc(FL + FL_MRG, lane);
        lds_wait_ge_fast(FL + FL_MRG, 4);
        {
            float mw[4], lw[4];
#pragma unroll
            for (int k = 0; k < 4; ++k) { mw[k] = ml[k * 16 + (c & 7)]; lw[k] = ml[k * 16 + 8 + (c & 7)]; }
            f32x4 ov[4];
#pragma unroll
            for (int k = 0; k < 4; ++k) ov[k] = *(const LAS f32x4*)(L + R2 + k * 8192 + ((2 * xi + (c >> 3)) * 32 + g * 8 + (c & 7)) * 16);
            const float M = fmaxf(fmaxf(mw[0], mw[1]), fmaxf(mw[2], mw[3]));
            float Lt = 0.f; f32x4 o = {0.f, 0.f, 0.f, 0.f};
#pragma unroll
            for (int k = 0; k < 4; ++k) { const float f = __builtin_amdgcn_exp2f((mw[k] - M) * C2); Lt = fmaf(f, lw[k], Lt); o += ov[k] * f; }
            __builtin_amdgcn_raw_buffer_store_b128(v4u{pk_bf16(o[0], o[1]), tag, pk_bf16(o[2], o[3]), tag}, rs,
                                                   OP_OFF + ((gid * 8 + sp) * 8 + (c & 7)) * 512 + (4 * (2 * xi + (c >> 3)) + g) * 16, 0, AUX_SC1);
            if (xi == 0 && lane < 8) __builtin_amdgcn_raw_buffer_store_b128(v4u{f2u(M), tag, f2u(Lt), tag}, rs, OM_OFF + ((gid * 8 + lane) * 8 + sp) * 16, 0, AUX_SC1);
        }
        {
            const int head = ks_o >> 1, dh = ks_o & 1, hk = head >> 3, hh = head & 7;
            const int bb = 2 * xi + (lane >> 5), sh = (lane >> 4) & 1, gidb = bb * 4 + hk;
            int vo[5];
#pragma unroll
            for (int i = 0; i < 4; ++i) vo[i] = OP_OFF + ((gidb * 8 + 4 * sh + i) * 8 + hh) * 512 + (16 * dh + c) * 16;
            vo[4] = OM_OFF + ((gidb * 8 + hh) * 8 + (lane & 7)) * 16;
            wait_until(tstart, MK_TO);
            v4u o5[5];
            gather<5>(rs, vo, tag, o5);
            const float ms = u2f(o5[4].x), ls = u2f(o5[4].z);
            float M = ms;
            M = fmaxf(M, dpp<XOR1>(M)); M = fmaxf(M, dpp<XOR2>(M)); M = fmaxf(M, dpp<XOR7>(M));
            const float we = __builtin_amdgcn_exp2f((ms - M) * C2);
            float Ls = we * ls;
            Ls += dpp<XOR1>(Ls); Ls += dpp<XOR2>(Ls); Ls += dpp<XOR7>(Ls);
            const float wn = we / Ls;
            LAS float* wl = (LAS float*)(L + ML_OFF + 256) + bb * 8;
            if ((lane & 31) < 8) wl[lane & 7] = wn;
            CBAR();
            const f32x4 wa = *(const LAS f32x4*)(wl + 4 * sh);
            float v0 = 0.f, v1 = 0.f, v2 = 0.f, v3 = 0.f;
#pragma unroll
            for (int i = 0; i < 4; ++i) {
                v0 = fmaf(wa[i], bf_lo(o5[i].x), v0); v1 = fmaf(wa[i], bf_hi(o5[i].x), v1);
                v2 = fmaf(wa[i], bf_lo(o5[i].z), v2); v3 = fmaf(wa[i], bf_hi(o5[i].z), v3);
            }
            v0 = x16_sum(v0); v1 = x16_sum(v1); v2 = x16_sum(v2); v3 = x16_sum(v3);
            if (sh == 0) *(LAS v2u*)(L + OA_OFF + bb * AC_STR + c * 8) = v2u{pk_bf16(v0, v1), pk_bf16(v2, v3)};
            lds_inc(FL + FL_O, lane);
        }
        gemm_b(L, R0, FL, g_wo + 4, FL + FL_O, OA_OFF, xi, lane, rs, SP_OFF + (nsl_o * 64 + ks_o) * 8192, tag);
        if (xi == 0) {
            const int nsl = bid & 3, j = bid >> 2, uo = (j >> 3) * 1024 + ((j & 7) * 8 + (lane & 7)) * 16;
            int vo[8];
#pragma unroll
            for (int i = 0; i < 8; ++i) vo[i] = SP_OFF + (nsl * 64 + 8 * i + (lane >> 3)) * 8192 + uo;
            wait_until(tstart, MK_TS);
            v4u f8[8];
            gather<8>(rs, vo, tag, f8);
            float sv[4] = {0.f, 0.f, 0.f, 0.f};
#pragma unroll
            for (int i = 0; i < 8; ++i) { sv[0] += bf_lo(f8[i].x); sv[1] += bf_hi(f8[i].x); sv[2] += bf_lo(f8[i].z); sv[3] += bf_hi(f8[i].z); }
#pragma unroll
            for (int q = 0; q < 4; ++q) { sv[q] += dpp<XOR8>(sv[q]); sv[q] = xrow16_sum(sv[q]); }
            if (lane < 8) *(LAS f32x4*)(L + ML_OFF + 512 + lane * 16) = f32x4{sv[0], sv[1], sv[2], sv[3]};
            lds_set(FL + FL_S, 1);
        }
    } else {
        const int yi = w - 5;
        lds_wait_ge(FL + FL_T, 1);
        unsigned tag = __builtin_amdgcn_readfirstlane(FL[FL_TV]);
        {
            const int bq = gid >> 2, hk = gid & 3;
            int vo[4];
#pragma unroll
            for (int i = 0; i < 4; ++i) vo[i] = QP_OFF + (i * 8 + bq) * QP_ROW + (hk * 256 + yi * 64 + lane) * 16;
            lds_wait_ge(FL + FL_QT, 4);
            wait_until(tstart, MK_TQ);
            __builtin_amdgcn_s_setprio(2);
            v4u q4[4];
            gather<4>(rs, vo, tag, q4);
            const float v0 = (bf_lo(q4[0].x) + bf_lo(q4[1].x)) + (bf_lo(q4[2].x) + bf_lo(q4[3].x));
            const float v1 = (bf_hi(q4[0].x) + bf_hi(q4[1].x)) + (bf_hi(q4[2].x) + bf_hi(q4[3].x));
            const float v2 = (bf_lo(q4[0].z) + bf_lo(q4[1].z)) + (bf_lo(q4[2].z) + bf_lo(q4[3].z));
            const float v3 = (bf_hi(q4[0].z) + bf_hi(q4[1].z)) + (bf_hi(q4[2].z) + bf_hi(q4[3].z));
            const int n = (yi * 64 + lane) * 4;
            *(LAS v2u*)(L + QS_OFF + (n >> 7) * QS_STR + (n & 127) * 2) = v2u{pk_bf16(v0, v1), pk_bf16(v2, v3)};
            lds_inc(FL + FL_Q, lane);
            __builtin_amdgcn_s_setprio(0);
        }
        if (has_w1) gemm_a(L, R3, FL, 16, nullptr, yi, bid & 3, lane, rs, HP_OFF + ((bid & 3) * 8 + (c & 7)) * HP_ROW + (bid >> 2) * 256, tag);
        else lds_wait_ge(FL + FL_LANDED, 14);
        if (has_w2) {
            const int bb = 2 * yi + (lane >> 5), ph = (lane >> 4) & 1;
            const v2u bi = *(const GAS v2u*)(p.b1 + ks_2 * 64 + 4 * c);
            int vo[2];
#pragma unroll
            for (int i = 0; i < 2; ++i) vo[i] = HP_OFF + ((2 * ph + i) * 8 + bb) * HP_ROW + ks_2 * 256 + c * 16;
            wait_until(tstart, MK_TH);
            __builtin_amdgcn_s_setprio(2);
            v4u h2[2];
            gather<2>(rs, vo, tag, h2);
            const float v0 = x16_sum(bf_lo(h2[0].x) + bf_lo(h2[1].x)) + bf_lo(bi.x), v1 = x16_sum(bf_hi(h2[0].x) + bf_hi(h2[1].x)) + bf_hi(bi.x);
            const float v2 = x16_sum(bf_lo(h2[0].z) + bf_lo(h2[1].z)) + bf_lo(bi.y), v3 = x16_sum(bf_hi(h2[0].z) + bf_hi(h2[1].z)) + bf_hi(bi.y);
            const v2u hv = {pk_bf16(gelu_tanh(v0), gelu_tanh(v1)), pk_bf16(gelu_tanh(v2), gelu_tanh(v3))};
            if (ph == 0) *(LAS v2u*)(L + HA_OFF + bb * AC_STR + c * 8) = hv;
            lds_inc(FL + FL_H, lane);
            gemm_b(L, R1, FL, g_w2 + 4, FL + FL_H, HA_OFF, yi, lane, rs, YP_OFF + (nsl_2 * 56 + ks_2) * 8192, tag);
        } else {
            lds_wait_ge(FL + FL_LANDED, g_wo + 4);
        }
        if (yi == 0) {
            const int nsl = bid & 3, j = bid >> 2, uo = (j >> 3) * 1024 + ((j & 7) * 8 + (lane & 7)) * 16;
            const int b = j >> 3, n = 256 * nsl + (j & 7) * 32 + 4 * (lane & 7);
            const v2u xx = *(const GAS v2u*)(p.x + b * D + n), bb = *(const GAS v2u*)(p.b2 + n);
            int vo[7];
#pragma unroll
            for (int i = 0; i < 7; ++i) vo[i] = YP_OFF + (nsl * 56 + 8 * i + (lane >> 3)) * 8192 + uo;
            wait_until(tstart, MK_TY);
            v4u f7[7];
            gather<7>(rs, vo, tag, f7);
            float yv[4] = {0.f, 0.f, 0.f, 0.f};
#pragma unroll
            for (int i = 0; i < 7; ++i) { yv[0] += bf_lo(f7[i].x); yv[1] += bf_hi(f7[i].x); yv[2] += bf_lo(f7[i].z); yv[3] += bf_hi(f7[i].z); }
#pragma unroll
            for (int q = 0; q < 4; ++q) { yv[q] += dpp<XOR8>(yv[q]); yv[q] = xrow16_sum(yv[q]); }
            const float g0 = silu(yv[0] + bf_lo(bb.x)), g1 = silu(yv[1] + bf_hi(bb.x)), g2 = silu(yv[2] + bf_lo(bb.y)), g3 = silu(yv[3] + bf_hi(bb.y));
            lds_wait_ge(FL + FL_S, 1);
            const f32x4 sv = *(const LAS f32x4*)(L + ML_OFF + 512 + (lane & 7) * 16);
            if (lane < 8)
                *(GAS v2u*)(p.out + b * D + n) = v2u{pk_bf16(bf_lo(xx.x) + sv[0] * g0, bf_hi(xx.x) + sv[1] * g1), pk_bf16(bf_lo(xx.y) + sv[2] * g2, bf_hi(xx.y) + sv[3] * g3)};
            if (bid == 0 && lane == 0) __hip_atomic_store((GAS unsigned*)(p.ws + WS_EPOCH), tag, __ATOMIC_RELAXED, __HIP_MEMORY_SCOPE_AGENT);
        }
    }
}

}

extern "C" void kernel_launch(void* const* d_in, const int* in_sizes, int n_in, void* d_out, int out_size, void* d_ws, size_t ws_size,
                              hipStream_t stream) {
    mk::Params p;
    p.x = (const u16*)d_in[0];
    p.kc = (const u16*)d_in[1];
    p.vc = (const u16*)d_in[2];
    p.wq = (const u16*)d_in[3];
    p.wo = (const u16*)d_in[4];
    p.w1 = (const u16*)d_in[6];
    p.b1 = (const u16*)d_in[7];
    p.w2 = (const u16*)d_in[8];
    p.b2 = (const u16*)d_in[9];
    p.out = (u16*)d_out;
    p.ws = (unsigned char*)d_ws;
    mk::fused_kernel<<<256, mk::NWAVE * 64, 0, stream>>>(p);
}
```
